```python
import torch

def _softmax(x, axis=-1):
    return torch.softmax(x, dim=axis)
def _gelu(x):
    return torch.nn.functional.gelu(x, approximate="tanh")
def _silu(x):
    return torch.nn.functional.silu(x)
def _sigmoid(x):
    return torch.sigmoid(x)
def _layernorm(x, axis=-1, eps=1e-5):
    m = x.mean(dim=axis, keepdim=True)
    v = x.var(dim=axis, keepdim=True, unbiased=False)
    return (x - m) / torch.sqrt(v + eps)
def _scatter_add(tgt, idx, src, axis=0):
    return tgt.clone().index_add_(axis, idx, src)
def _segment_sum(src, seg, n):
    out = torch.zeros((n,) + src.shape[1:], dtype=src.dtype)
    return out.index_add_(0, seg, src)
def _pad(x, pw):
    flat = tuple(v for pair in reversed(pw) for v in pair)
    return torch.nn.functional.pad(x, flat)

kernel_name = 'gated_mix__mha_block__geglu_ffn__prompt'

def setup_inputs():

    _w = (lambda a: (lambda t: t.to(torch.bfloat16) if t.is_floating_point() else t)(torch.as_tensor(a)))
    def _A_setup():
        B, S, D, H = 1, 2048, 448, 16
        return {
            "x": _w((lambda *s: torch.randn(*s, dtype=torch.bfloat16) * 0.05)(B, S, D)),
            "wqkv": _w((lambda *s: torch.randn(*s, dtype=torch.bfloat16) * 0.8)(D, 3 * D)),
            "wo": _w((lambda *s: torch.randn(*s, dtype=torch.bfloat16) * 0.05)(D, D)),
        }
    _A_in = _A_setup()

    def _B_setup():
        B, S, D, F = 1, 2048, 448, 1792
        return {
            "x": _w((lambda *s: torch.randn(*s, dtype=torch.bfloat16) * 0.05)(B, S, D)),
            "w_gate": _w((lambda *s: torch.randn(*s, dtype=torch.bfloat16) * 0.1)(D, F)),
            "w_up": _w((lambda *s: torch.randn(*s, dtype=torch.bfloat16) * 0.1)(D, F)),
            "w_down": _w((lambda *s: torch.randn(*s, dtype=torch.bfloat16) * 0.1)(F, D)),
        }
    _B_in = _B_setup()

    return {
        "A_x": _A_in["x"],
        "A_wqkv": _A_in["wqkv"],
        "A_wo": _A_in["wo"],
        "B_x": _B_in["x"],
        "B_w_gate": _B_in["w_gate"],
        "B_w_up": _B_in["w_up"],
        "B_w_down": _B_in["w_down"],
        "gate_w": _w((lambda *s: torch.randn(*s, dtype=torch.bfloat16) * 0.05)(448) * 16.0),
    }

def reference(A_x, A_wqkv, A_wo, B_x, B_w_gate, B_w_up, B_w_down, gate_w):
    def _A_ref(x, wqkv, wo):
        B, S, D = x.shape
        H = 16
        Dh = D // H
        qkv = (lambda x, *d: x.permute(*d))((x @ wqkv).reshape(B, S, 3, H, Dh), 2, 0, 3, 1, 4)
        q, k, v = qkv[0], qkv[1], qkv[2]
        scores = torch.einsum("bhqd,bhkd->bhqk", q, k) * (Dh ** -0.5)
        p = _softmax(scores, axis=-1)
        o = torch.einsum("bhqk,bhkd->bhqd", p, v)
        return (lambda x, *d: x.permute(*d))(o, 0, 2, 1, 3).reshape(B, S, D) @ wo

    def _B_ref(x, w_gate, w_up, w_down):
        return (_gelu(x @ w_gate) * (x @ w_up)) @ w_down

    x = A_x
    g = _sigmoid((x * gate_w).sum(-1, keepdims=True))
    a = _A_ref(x, A_wqkv, A_wo)
    b = _B_ref(x, B_w_gate, B_w_up, B_w_down)
    return g * a + (1 - g) * b
```

```cpp
#include <hip/hip_runtime.h>
#include <hip/hip_bf16.h>
typedef unsigned short u16;
__device__ __forceinline__ float b2f(u16 v){ return __uint_as_float(((unsigned)v)<<16); }
__device__ __forceinline__ u16 f2b(float f){ unsigned u=__float_as_uint(f); u += 0x7fff + ((u>>16)&1); return (u16)(u>>16); }
constexpr int S=2048, D=448, H=16, DH=28, F=1792;
__global__ void gemm_naive(const u16* A, const u16* B, float* C, int M, int N, int K){
  int n = blockIdx.x*64 + (threadIdx.x&63); int m = blockIdx.y*4 + (threadIdx.x>>6);
  if(n>=N||m>=M) return;
  float acc=0; for(int k=0;k<K;k++) acc += b2f(A[m*K+k])*b2f(B[k*N+n]);
  C[m*N+n]=acc;
}
__global__ void tobf(const float* a, u16* b, int n){ int i=blockIdx.x*256+threadIdx.x; if(i<n) b[i]=f2b(a[i]); }
__global__ void geglu(const float* g, const float* u, u16* h, int n){ int i=blockIdx.x*256+threadIdx.x; if(i<n){ float x=g[i]; float t=tanhf(0.7978845608028654f*(x+0.044715f*x*x*x)); h[i]=f2b(0.5f*x*(1.f+t)*u[i]); } }
__global__ void attn_naive(const u16* qkv, u16* o){
  int w = blockIdx.x*4 + (threadIdx.x>>6); int lane = threadIdx.x&63; int h = w / S, q = w % S;
  float qv[DH]; for(int d=0;d<DH;d++) qv[d]=b2f(qkv[q*3*D + h*DH + d]);
  float sc[S/64]; float m=-1e30f;
  for(int i=0;i<S/64;i++){ int k=i*64+lane; float s=0; for(int d=0;d<DH;d++) s+=qv[d]*b2f(qkv[k*3*D + D + h*DH + d]); s*=0.1889822365f; sc[i]=s; m=fmaxf(m,s);}
  for(int off=32;off;off>>=1) m=fmaxf(m,__shfl_xor(m,off));
  float l=0; float acc[DH]; for(int d=0;d<DH;d++) acc[d]=0;
  for(int i=0;i<S/64;i++){ int k=i*64+lane; float p=__expf(sc[i]-m); l+=p; for(int d=0;d<DH;d++) acc[d]+=p*b2f(qkv[k*3*D+2*D+h*DH+d]); }
  for(int off=32;off;off>>=1){ l+=__shfl_xor(l,off); for(int d=0;d<DH;d++) acc[d]+=__shfl_xor(acc[d],off);}
  if(lane==0) for(int d=0;d<DH;d++) o[q*D+h*DH+d]=f2b(acc[d]/l);
}
__global__ void final_k(const u16* x, const u16* gw, const float* a, const float* b, u16* out){
  int m = blockIdx.x; __shared__ float red[64]; int t=threadIdx.x; float s=0; for(int k=t;k<D;k+=64) s+=b2f(x[m*D+k])*b2f(gw[k]);
  for(int off=32;off;off>>=1) s+=__shfl_xor(s,off);
  float g=1.f/(1.f+__expf(-s));
  for(int n=t;n<D;n+=64) out[m*D+n]=f2b(g*a[m*D+n]+(1.f-g)*b[m*D+n]);
}
extern "C" void kernel_launch(void* const* d_in, const int* in_sizes, int n_in, void* d_out, int out_size, void* d_ws, size_t ws_size, hipStream_t stream){
  const u16* x=(const u16*)d_in[0]; const u16* wqkv=(const u16*)d_in[1]; const u16* wo=(const u16*)d_in[2];
  const u16* wg=(const u16*)d_in[4]; const u16* wu=(const u16*)d_in[5]; const u16* wd=(const u16*)d_in[6]; const u16* gw=(const u16*)d_in[7];
  char* ws=(char*)d_ws;
  float* t0=(float*)ws; float* t1=(float*)(ws+(32<<20)); u16* qkv=(u16*)(ws+(64<<20)); u16* o=(u16*)(ws+(80<<20)); u16* h=(u16*)(ws+(96<<20)); float* a=(float*)(ws+(112<<20)); float* b=(float*)(ws+(128<<20));
  gemm_naive<<<dim3(1344/64,S/4),256,0,stream>>>(x,wqkv,t0,S,1344,D);
  tobf<<<(S*1344+255)/256,256,0,stream>>>(t0,qkv,S*1344);
  attn_naive<<<H*S/4,256,0,stream>>>(qkv,o);
  gemm_naive<<<dim3(7,S/4),256,0,stream>>>(o,wo,a,S,D,D);
  gemm_naive<<<dim3(F/64,S/4),256,0,stream>>>(x,wg,t0,S,F,D);
  gemm_naive<<<dim3(F/64,S/4),256,0,stream>>>(x,wu,t1,S,F,D);
  geglu<<<(S*F+255)/256,256,0,stream>>>(t0,t1,h,S*F);
  gemm_naive<<<dim3(7,S/4),256,0,stream>>>(h,wd,b,S,D,F);
  final_k<<<S,64,0,stream>>>(x,gw,a,b,(u16*)d_out);
}
```

```cpp
#include <hip/hip_runtime.h>
#include <hip/hip_bf16.h>
#include <stdint.h>
#include <type_traits>

typedef unsigned long long u64;
using bf16x8 = __attribute__((ext_vector_type(8))) short;
using s16x4 = __attribute__((ext_vector_type(4))) short;
using f32x16 = __attribute__((ext_vector_type(16))) float;
using f32x4 = __attribute__((ext_vector_type(4))) float;
using f32x2 = __attribute__((ext_vector_type(2))) float;
using u32x4 = __attribute__((ext_vector_type(4))) unsigned;
using u32x2 = __attribute__((ext_vector_type(2))) unsigned;
using bf2 = __attribute__((ext_vector_type(2))) __bf16;
#define LAS __attribute__((address_space(3)))
#define GAS __attribute__((address_space(1)))
typedef LAS const char* lptr;
#define DI __device__ __forceinline__
#define MFMA(a, b, c) __builtin_amdgcn_mfma_f32_32x32x16_bf16((a), (b), (c), 0, 0, 0)
#define SBAR() __builtin_amdgcn_sched_barrier(0)

constexpr unsigned WS_EPOCH = 0, WS_FW = 4096, WS_FKV = 8192, WS_FH = 24576, WS_FO = 40960, WS_G = 49152, WS_WP = 65536, WS_KV = 0x180000, WS_H = 0x580000,
                   WS_O = 0xC80000, WS_END = 0xE80000, WS_DBG = 0x1000000;
constexpr u64 TAG_STEP = 0x9E3779B97F4A7C15ull;
constexpr int LDS_BYTES = 163840;
constexpr unsigned K1 = 1024, L_WP = 0, L_RING = 48 * K1, L_QX = 80 * K1, L_COMB = 80 * K1, L_RED = 80 * K1;
constexpr float QSCALE = 0.18898223650461362f * 1.4426950408889634f;
constexpr unsigned SPIN_CAP = 1u << 20;

template <bool SC1> DI void dma(unsigned voff, const char* sbase, unsigned lds) {
    if (SC1) asm volatile("s_mov_b32 m0, %2\n\ts_nop 0\n\tglobal_load_lds_dwordx4 %0, %1 sc1" ::"v"(voff), "s"(sbase), "s"(lds) : "memory");
    else asm volatile("s_mov_b32 m0, %2\n\ts_nop 0\n\tglobal_load_lds_dwordx4 %0, %1" ::"v"(voff), "s"(sbase), "s"(lds) : "memory");
}
template <int N> DI void wait_vm() { asm volatile("s_waitcnt vmcnt(%0)" ::"i"(N) : "memory"); }
template <int N> DI void wait_bar() { asm volatile("s_waitcnt vmcnt(%0) lgkmcnt(0)\n\ts_barrier" ::"i"(N) : "memory"); }
DI void lds_bar() { asm volatile("s_waitcnt lgkmcnt(0)\n\ts_barrier" ::: "memory"); }
DI bf16x8 rd128(lptr p) { return *(const LAS bf16x8*)p; }
typedef short v4i16_t __attribute__((ext_vector_type(4)));
DI s16x4 rdtr(lptr p) { return __builtin_bit_cast(s16x4, __builtin_amdgcn_ds_read_tr16_b64_v4i16((LAS v4i16_t*)p)); }
DI bf16x8 rdtr2(lptr p, int d) { const s16x4 a = rdtr(p), b = rdtr(p + d); return __builtin_shufflevector(a, b, 0, 1, 2, 3, 4, 5, 6, 7); }
DI unsigned pk(float lo, float hi) { const f32x2 v = {lo, hi}; return __builtin_bit_cast(unsigned, __builtin_convertvector(v, bf2)); }
DI float swap_sum(float v) { auto r = __builtin_amdgcn_permlane32_swap(__float_as_uint(v), __float_as_uint(v), false, false); return __uint_as_float(r[0]) + __uint_as_float(r[1]); }
DI float swap_max(float v) { auto r = __builtin_amdgcn_permlane32_swap(__float_as_uint(v), __float_as_uint(v), false, false); return __builtin_fmaxf(__uint_as_float(r[0]), __uint_as_float(r[1])); }
DI float from_hi(float v) { auto r = __builtin_amdgcn_permlane32_swap(__float_as_uint(v), __float_as_uint(v), false, false); return __uint_as_float(r[1]); }
DI void st128(char* ws, unsigned off, unsigned voff, u32x4 v) {
    asm volatile("global_store_dwordx4 %0, %1, %2 sc1\n\ts_nop 1" ::"v"(voff), "v"(v), "s"(ws + off) : "memory");
}
DI u64 flag_load(const char* ws, unsigned voff) { u64 v; asm volatile("global_load_dwordx2 %0, %1, %2 sc1" : "=v"(v) : "v"(voff), "s"(ws) : "memory"); return v; }
DI u64 make_tag(u64 ep) {
    unsigned lo, hi; asm volatile("v_readfirstlane_b32 %0, %2\n\tv_readfirstlane_b32 %1, %3\n\ts_nop 0" : "=&s"(lo), "=s"(hi) : "v"((unsigned)ep), "v"((unsigned)(ep >> 32)) : "memory");
    return (((u64)hi << 32) | lo) + TAG_STEP;
}
DI bool flags_set(u64 f, u64 tag) { u64 m; asm volatile("v_cmp_ne_u64 %0, %1, %2\n\ts_nop 0" : "=s"(m) : "s"(tag), "v"(f) : "memory"); return m == 0; }
DI void poll2(const char* ws, unsigned voff, u64 tag) {
#pragma nounroll
    for (unsigned spins = 0; spins < SPIN_CAP; ++spins) {
        u32x4 v; asm volatile("global_load_dwordx4 %0, %1, %2 sc1\n\ts_waitcnt vmcnt(0)" : "=&v"(v) : "v"(voff), "s"(ws) : "memory");
        const unsigned lo = (unsigned)tag, hi = (unsigned)(tag >> 32);
        if (__all(v[0] == lo && v[1] == hi && v[2] == lo && v[3] == hi)) break;
        __builtin_amdgcn_s_sleep(1);
    }
}
DI void poll(const char* ws, unsigned voff, u64 tag) {
#pragma nounroll
    for (unsigned spins = 0; spins < SPIN_CAP; ++spins) {
        u64 v; asm volatile("global_load_dwordx2 %0, %1, %2 sc1\n\ts_waitcnt vmcnt(0)" : "=&v"(v) : "v"(voff), "s"(ws) : "memory");
        if (__all(v == tag)) break;
        __builtin_amdgcn_s_sleep(1);
    }
}
DI void set_flag(char* ws, unsigned off, u64 tag, int lane) {
    if (lane == 0) __hip_atomic_store((GAS u64*)(ws + off), tag, __ATOMIC_RELAXED, __HIP_MEMORY_SCOPE_AGENT);
}
DI u32x4 geglu(const f32x16& a) {
    constexpr float C1 = -2.f * 0.7978845608028654f * 1.4426950408889634f, C2 = C1 * 0.044715f;
    u32x4 o;
#pragma unroll
    for (int i = 0; i < 4; ++i) {
        const f32x2 g = {a[2 * i], a[2 * i + 1]}, u = {a[8 + 2 * i], a[9 + 2 * i]};
        const f32x2 t = g * g * C2 + C1, z = t * g;
        const f32x2 d = f32x2{__builtin_amdgcn_exp2f(z[0]), __builtin_amdgcn_exp2f(z[1])} + 1.f;
        const f32x2 h = g * u * f32x2{__builtin_amdgcn_rcpf(d[0]), __builtin_amdgcn_rcpf(d[1])};
        o[i] = pk(h[0], h[1]);
    }
    return o;
}

#define STAMP(i) do {} while (0)

template <int N> using IC = std::integral_constant<int, N>;

__global__ __launch_bounds__(512, 2) void fused_kernel(const char* __restrict__ x, const char* __restrict__ wqkv, const char* __restrict__ wo, const char* __restrict__ wg,
                                                       const char* __restrict__ wu, const char* __restrict__ wd, const char* __restrict__ gw, char* __restrict__ out, char* ws) {
    extern __shared__ __attribute__((aligned(16))) char smem[];
    const int tid = threadIdx.x, lane = tid & 63, w = __builtin_amdgcn_readfirstlane(tid >> 6), t = w & 3, cls = w >> 2;
    const int r31 = lane & 31, hh = lane >> 5, g16 = lane >> 4, q4 = (lane >> 2) & 3, p4 = lane & 3;
    const int b = blockIdx.x, x8 = b & 7, j = b >> 3, hg = x8 & 3, head = 4 * hg + (j & 3), r = 8 * (x8 >> 2) + (j >> 2);
    const bool busy = head < 14;
    const int fi = (x8 >> 2) * 112 + hg * 32 + (hg < 3 ? j : (j >> 2) * 2 + (j & 1)), ft = fi / 7, fc = fi - 7 * ft;
    const unsigned L0 = (unsigned)(uintptr_t)smem;
    const lptr lds = (lptr)smem;
    const __amdgpu_buffer_rsrc_t rs = __builtin_amdgcn_make_buffer_rsrc(ws, 0, WS_END, 0x00020000);
    const unsigned l16 = lane * 16;
    const f32x16 zero16 = {};
    STAMP(0);

    const u64 ep = flag_load(ws, WS_EPOCH);
    const unsigned vx0 = (lane >> 3) * 896 + (((lane & 7) ^ g16) << 4), vx1 = (8 + (lane >> 3)) * 896 + (((lane & 7) ^ (4 + g16)) << 4);
    const unsigned csw = (lane & 7) ^ ((g16 & 1) << 2);
    const unsigned vg = (lane >> 3) * 3584 + (csw << 4), vu = (lane >> 3) * 3584 + ((csw ^ 2) << 4), vd = (lane >> 3) * 896 + (csw << 4);
    const char* xs = x + (size_t)(128 * r + 16 * w) * 896;
    const char* wgs = wg + (8 * w) * 3584 + head * 256;
    const char* wus = wu + (8 * w) * 3584 + head * 256;
    auto dma_x = [&](int i, int e, unsigned dst) { dma<false>(e ? vx1 : vx0, xs + 128 * i, L0 + dst + 2048 * w + 1024 * e); };
    auto dma_gu = [&](int i, int pass, int e, unsigned dst) { dma<false>(e ? vu : vg, (e ? wus : wgs) + i * (64 * 3584) + pass * 128, L0 + dst + 8192 * e + 1024 * w); };
    auto dma_pair = [&](int i, int e) { if (e < 2) dma_x(i, e, (i & 3) * 32768); else dma_gu(i, 0, e - 2, (i & 3) * 32768 + 16384); };
    auto gub_at = [](int i) { return i < 2 ? 128 * K1 + i * 16384 : i < 4 ? 96 * K1 + (i - 2) * 16384 : (i - 4) * 16384; };
    const char* wps = ws + WS_WP + head * (84 * 1024) + w * 1024;
    auto dma_wp = [&](int m) { if (m < 10 || cls == 0) dma<true>(l16, wps + m * 8192, L0 + L_WP + w * 1024 + m * 8192); };
    const unsigned xm = (r31 >> 1) & 7;
    const lptr xb = lds + (32 * t + r31) * 128;
    const lptr gub = lds + (g16 & 1) * 8192 + (8 * (g16 >> 1) + q4) * 128 + (((4 * cls + (p4 >> 1)) ^ (((q4 >> 1) & 1) << 2) ^ ((g16 & 1) << 1)) << 4) + 8 * (p4 & 1);
    const lptr gub1 = (lptr)((unsigned)(uintptr_t)gub ^ 32u);
    const unsigned fw_off = WS_FW + (lane < 28 ? lane : 0) * 8;

    bf16x8 xf[28];
    f32x16 a0 = zero16, a1 = zero16;
    u64 tag;
    if (busy) {
#pragma unroll
        for (int e = 0; e < 4; ++e) dma_pair(0, e);
        asm volatile("s_barrier" ::: "memory");
#pragma unroll
        for (int e = 0; e < 8; ++e) dma_pair(1 + (e >> 2), e & 3);
        wait_bar<8>();
        tag = make_tag(ep);
        STAMP(1);
        bf16x8 wa = rdtr2(gub + 16384, 512), wb = rdtr2(gub1 + 16384, 512);
        xf[0] = rd128(xb + ((hh ^ xm) << 4));
        dma_pair(3, 0);
#pragma unroll
        for (int s = 0; s < 28; ++s) {
            if ((s & 3) == 3 && s < 27) {
                if (s == 23) { poll(ws, fw_off, tag); lds_bar(); } else wait_bar<8>();
            }
            const bf16x8 ca = wa, cb = wb;
            if (s < 27) {
                const int n = s + 1, sl = ((n >> 2) & 3) * 32768, ks = n & 3;
                wa = rdtr2(gub + sl + 16384 + ks * 2048, 512); wb = rdtr2(gub1 + sl + 16384 + ks * 2048, 512);
                xf[n] = rd128(xb + sl + (((2 * ks + hh) ^ xm) << 4));
            }
            a0 = MFMA(ca, xf[s], a0);
            SBAR();
            if (s < 15) dma_pair(3 + ((s + 1) >> 2), (s + 1) & 3);
            else if (s < 23) dma_gu((s - 15) >> 1, 1, (s - 15) & 1, gub_at((s - 15) >> 1));
            else if (s < 27) { dma_wp(2 * (s - 23)); dma_wp(2 * (s - 23) + 1); }
            SBAR();
            a1 = MFMA(cb, xf[s], a1);
        }
        wait_bar<5>();
    } else {
        const int sp = (head - 14) + 2 * r;
        if (sp < 28) {
            const u32x4 gwf = *(const u32x4*)(gw + 32 * sp + 16 * hh);
            const char* src = wqkv + (size_t)sp * 43008;
#pragma unroll
            for (int m = 0; m < 6; ++m) if (m < 5 || w < 2) dma<false>(l16, src + (m * 8 + w) * 1024, L0 + 112 * K1 + (m * 8 + w) * 1024);
            wait_bar<0>();
            tag = make_tag(ep);
            const lptr sb = lds + 112 * K1 + (8 * (g16 >> 1) + q4) * 2688 + (16 * (g16 & 1) + 4 * p4) * 2 + w * 112;
            u32x4 f[6];
#pragma unroll
            for (int e = 0; e < 6; ++e) f[e] = __builtin_bit_cast(u32x4, rdtr2(sb + (e % 3) * 896 + (e / 3) * 56, 4 * 2688));
#pragma unroll
            for (int e = 0; e < 6; ++e) {
                if (r31 >= 28) f[e] = (e % 3 == 0 && r31 == 28) ? gwf : u32x4{0, 0, 0, 0};
                st128(ws, WS_WP + (((2 * w + e / 3) * 28 + sp) * 3 + e % 3) * 1024, l16, f[e]);
            }
            wait_bar<0>();
            if (w == 0) set_flag(ws, WS_FW + sp * 8, tag, lane);
        } else {
            wait_vm<0>();
            tag = make_tag(ep);
        }
        STAMP(1);
#pragma unroll
        for (int e = 0; e < 14; ++e) dma_x(e >> 1, e & 1, (e >> 1) * 16384);
        wait_bar<0>();
#pragma unroll
        for (int s = 0; s < 28; ++s) xf[s] = rd128(xb + (s >> 2) * 16384 + (((2 * (s & 3) + hh) ^ xm) << 4));
        poll(ws, fw_off, tag);
        lds_bar();
#pragma unroll
        for (int m = 0; m < 8; ++m) dma_wp(m);
        wait_bar<5>();
    }
    STAMP(2);
    const lptr wpb = lds + L_WP + l16;
    const int tile = 4 * r + t;
    const unsigned kv_off = WS_KV + (head * 64 + tile) * 4096;
    const unsigned h_off = WS_H + (tile * 112 + head * 8 + 2 * cls) * 1024;
    bf16x8 qf0, qf1;
    float gate = 0.f;
    if (cls == 0) {
        f32x16 aq = zero16, ak = zero16;
        bf16x8 fq = rd128(wpb), fk = rd128(wpb + 1024);
#pragma unroll
        for (int s = 0; s < 28; ++s) {
            if (s == 7) wait_bar<3>();
            if (s == 20) wait_bar<0>();
            const bf16x8 cq = fq, ck = fk;
            if (s < 27) { fq = rd128(wpb + (s + 1) * 3072); fk = rd128(wpb + (s + 1) * 3072 + 1024); }
            aq = MFMA(cq, xf[s], aq);
            if (s < 3) { SBAR(); dma_wp(8 + s); SBAR(); }
            ak = MFMA(ck, xf[s], ak);
        }
        lds_bar();
        if (hh) ak[12] = 1.f;
        const u32x4 k0 = {pk(ak[0], ak[1]), pk(ak[2], ak[3]), pk(ak[4], ak[5]), pk(ak[6], ak[7])}, k1 = {pk(ak[8], ak[9]), pk(ak[10], ak[11]), pk(ak[12], ak[13]), pk(ak[14], ak[15])};
        st128(ws, kv_off, l16, k0); st128(ws, kv_off + 1024, l16, k1);
        const float logit = from_hi(aq[12]);
        gate = __builtin_amdgcn_rcpf(1.f + __builtin_amdgcn_exp2f(-1.4426950408889634f * logit));
        if (head == 0) asm volatile("global_store_dword %0, %1, %2 sc1" ::"v"(r31 * 4), "v"(gate), "s"(ws + WS_G + tile * 128) : "memory");
        aq *= QSCALE;
        if (hh) aq[12] = 0.f;
        const u32x4 q0 = {pk(aq[0], aq[1]), pk(aq[2], aq[3]), pk(aq[4], aq[5]), pk(aq[6], aq[7])};
        u32x4 q1 = {pk(aq[8], aq[9]), pk(aq[10], aq[11]), pk(aq[12], aq[13]), pk(aq[14], aq[15])};
        f32x16 sc = MFMA(__builtin_bit_cast(bf16x8, k0), __builtin_bit_cast(bf16x8, q0), zero16);
        sc = MFMA(__builtin_bit_cast(bf16x8, k1), __builtin_bit_cast(bf16x8, q1), sc);
        u32x4 h0 = {}, h1 = {};
        if (busy) h0 = geglu(a0);
        float m = sc[0];
#pragma unroll
        for (int i = 1; i < 16; ++i) m = __builtin_fmaxf(m, sc[i]);
        m = swap_max(m);
        if (hh) q1[2] = pk(-m, 0.f);
        qf0 = __builtin_bit_cast(bf16x8, q0); qf1 = __builtin_bit_cast(bf16x8, q1);
        *(LAS u32x4*)(smem + L_QX + t * 2048 + l16) = q0; *(LAS u32x4*)(smem + L_QX + t * 2048 + 1024 + l16) = q1;
        wait_vm<0>();
        set_flag(ws, WS_FKV + (head * 128 + tile * 2) * 8, tag, lane);
        if (busy) { st128(ws, h_off, l16, h0); h1 = geglu(a1); st128(ws, h_off + 1024, l16, h1); }
        lds_bar();
    } else {
        dma_wp(8); dma_wp(9);
        if (busy) st128(ws, h_off, l16, geglu(a0));
        if (busy) wait_bar<3>(); else wait_bar<2>();
        if (busy) st128(ws, h_off + 1024, l16, geglu(a1));
        f32x16 av = zero16;
        bf16x8 fv = rd128(wpb + 2048);
#pragma unroll
        for (int s = 0; s < 28; ++s) {
            if (s == 8) wait_bar<0>();
            const bf16x8 cv = fv;
            if (s < 27) fv = rd128(wpb + (s + 1) * 3072 + 2048);
            av = MFMA(xf[s], cv, av);
        }
        lds_bar();
        u32x4 v0 = {pk(av[4], av[0]), pk(av[5], av[1]), pk(av[6], av[2]), pk(av[7], av[3])}, v1 = {pk(av[12], av[8]), pk(av[13], av[9]), pk(av[14], av[10]), pk(av[15], av[11])};
        if (r31 == 28) { v0 = u32x4{0x3F803F80u, 0x3F803F80u, 0x3F803F80u, 0x3F803F80u}; v1 = v0; }
        st128(ws, kv_off + 2048, l16, v0); st128(ws, kv_off + 3072, l16, v1);
        if (!busy) { wait_vm<0>(); set_flag(ws, WS_FKV + (head * 128 + tile * 2 + 1) * 8, tag, lane); lds_bar(); }
    }
    STAMP(3);
    if (busy) {
        a0 = zero16; a1 = zero16;
        bf16x8 wa = rdtr2(gub + gub_at(0), 512), wb = rdtr2(gub1 + gub_at(0), 512);
#pragma unroll
        for (int s = 0; s < 28; ++s) {
            if (s == 8 && cls == 1) { wait_vm<8>(); set_flag(ws, WS_FKV + (head * 128 + tile * 2 + 1) * 8, tag, lane); }
            if (s == 15 && cls == 1) wait_bar<0>();
            const bf16x8 ca = wa, cb = wb;
            if (s < 27) { const int o_ = gub_at((s + 1) >> 2) + ((s + 1) & 3) * 2048; wa = rdtr2(gub + o_, 512); wb = rdtr2(gub1 + o_, 512); }
            a0 = MFMA(ca, xf[s], a0);
            if (s < 12) {
                SBAR();
                if (cls == 1) { const int i = 4 + s / 4, e = s & 1, up = (s >> 1) & 1;
                    dma<false>(e ? vu : vg, (e ? wus : wgs) + i * (64 * 3584) + 128 - up * (32 * 3584), L0 + gub_at(i) + 8192 * e + 1024 * w - up * 4096); }
                SBAR();
            }
            a1 = MFMA(cb, xf[s], a1);
        }
        st128(ws, h_off + 4096, l16, geglu(a0));
        st128(ws, h_off + 5120, l16, geglu(a1));
    }
    STAMP(4);
    const char* kvs = ws + WS_KV + head * (64 * 4096) + cls * (32 * 4096) + t * 2048;
    auto dma_kv = [&](int s, int e) { dma<true>(l16, kvs + s * 8192 + e * 1024, L0 + L_RING + (s & 3) * 16384 + cls * 8192 + t * 2048 + e * 1024); };
    if (cls == 1) {
        qf0 = rd128(lds + L_QX + t * 2048 + l16); qf1 = rd128(lds + L_QX + t * 2048 + 1024 + l16);
        poll2(ws, WS_FKV + head * 1024 + l16, tag);
        const char* src = ws + WS_KV + head * (64 * 4096) + t * 2048;
#pragma unroll
        for (int e = 0; e < 8; ++e) {
            const int s = e >> 2, kh = (e >> 1) & 1;
            dma<true>(l16, src + kh * (32 * 4096) + s * 8192 + (e & 1) * 1024, L0 + L_RING + s * 16384 + kh * 8192 + t * 2048 + (e & 1) * 1024);
            if (e == 3 && busy) set_flag(ws, WS_FH + (tile * 32 + head * 2 + 1) * 8, tag, lane);
        }
        wait_bar<4>();
    } else {
        if (busy) { wait_vm<0>(); set_flag(ws, WS_FH + (tile * 32 + head * 2) * 8, tag, lane); }
        lds_bar();
    }
    STAMP(5);
    const int kq = t, tt = cls, T2 = 2 * ft + tt;
    const char* wds = wd + (size_t)fc * 128;
    auto dma_wd = [&](int stage, int kq_, int pc, unsigned dst) { dma<false>(vd, wds + (size_t)(448 * kq_ + 64 * stage + 8 * pc) * 896, L0 + dst + pc * 1024); };
    const unsigned dk = kq * 8192, d2 = kq < 2 ? 32 * K1 + dk : 128 * K1 + dk;
    u32x4 hf[28];
    float g2 = 0.f;
    f32x16 o = zero16;
    {
        const lptr rb = lds + L_RING + cls * 8192 + l16;
        const unsigned fh_off = lane < 28 ? WS_FH + (T2 * 32 + lane) * 8 : WS_FKV + T2 * 16;
        u64 fh = 0;
        auto vm = [&](auto kc, auto ic) {
            constexpr int k = decltype(kc)::value, i = decltype(ic)::value;
            if constexpr (k == 3 && i == 0) fh = flag_load(ws, fh_off);
            if constexpr (i < 2) {
                if constexpr (k + 2 <= 15) dma_kv(k + 2, i);
                else if constexpr (k <= 15) { const int kq_ = 2 * (k - 14) + cls; dma_wd(3, kq_, 2 * t + i, 48 * K1 + kq_ * 8192); }
            } else if constexpr (k <= 3) {
                if constexpr (i < 5) { constexpr int n = 3 * k + i - 2; dma_wd(n >> 2, kq, 4 * tt + (n & 3), n < 4 ? dk : n < 8 ? 112 * K1 + dk : d2); }
            } else if constexpr (k == 4) {
            } else if constexpr (k <= 14) {
                constexpr int n = 3 * (k - 5) + i - 2;
                if constexpr (i < 5 && n < 28) hf[n] = __builtin_amdgcn_raw_buffer_load_b128(rs, l16, WS_H + (T2 * 112 + 28 * kq + n) * 1024, 16);
                if constexpr (i < 5 && n == 28) g2 = __uint_as_float(__builtin_amdgcn_raw_buffer_load_b32(rs, r31 * 4, WS_G + T2 * 128, 16));
            }
        };
        auto bar = [&](auto kc) {
            constexpr int k = decltype(kc)::value;
            auto ex = [](int s) { return s < 0 ? 0 : s <= 3 ? 3 : s == 4 ? 0 : s <= 13 ? 3 : s == 14 ? 2 : 0; };
            if constexpr (k == 1) wait_vm<5>();
            else if constexpr (k <= 15) wait_vm<ex(k - 2) + 2 + ex(k - 1) + (k == 4 ? 1 : 0)>();
            asm volatile("s_barrier" ::: "memory");
            if constexpr (k == 5) { if (!flags_set(fh, tag)) poll(ws, fh_off, tag); }
        };
        bf16x8 kf0 = rd128(rb), kf1 = rd128(rb + 1024), vf0, vf1;
        vm(IC<0>{}, IC<0>{});
        f32x16 sa = MFMA(kf0, qf0, zero16), sb;
        sa = MFMA(kf1, qf1, sa);
        kf0 = rd128(rb + 4096); kf1 = rd128(rb + 4096 + 1024);
        vm(IC<0>{}, IC<1>{}); vm(IC<0>{}, IC<2>{});
        sb = zero16;
        SBAR();
        asm volatile("s_nop 15" ::: "memory");
#define SDWA_LO " dst_sel:WORD_0 dst_unused:UNUSED_PRESERVE src0_sel:WORD_1\n\t"
#define GRP(C, i0, MF, FA, FB)                                                                                                                        \
    asm volatile("v_exp_f32 v[%c5], v[%c5]\n\tv_exp_f32 v[%c5+4], v[%c5+4]\n\t" MF "\n\tv_exp_f32 v[%c5+1], v[%c5+1]\n\tv_exp_f32 v[%c5+5], v[%c5+5]\n\t"  \
                 "v_mov_b32_sdwa v[%c5], v[%c5+4]" SDWA_LO "v_mov_b32_sdwa v[%c5+1], v[%c5+5]" SDWA_LO                                               \
                 : "+{v[224:239]}"(sa), "+{v[240:255]}"(sb), "+v"(o) : "v"(FA), "v"(FB), "i"((C) + (i0)), "i"(C), "i"(464 - (C)))
#define MF_PV1 "v_mfma_f32_32x32x16_bf16 %2, %3, v[%c7+8:%c7+11], %2"
#define MF_QK0 "v_mfma_f32_32x32x16_bf16 v[%c7:%c7+15], %3, %4, 0"
#define MF_QK1 "v_mfma_f32_32x32x16_bf16 v[%c7:%c7+15], %3, %4, v[%c7:%c7+15]"
#define MF_PV0 "v_mfma_f32_32x32x16_bf16 %2, %3, v[%c6:%c6+3], %2"
#define UNIT(C, u, H1, H2, H3, H4)                                                                                                                    \
    {                                                                                                                                                 \
        const int so = (((u) >> 1) & 3) * 16384 + ((u) & 1) * 4096, sn = ((((u) + 2) >> 1) & 3) * 16384 + ((u) & 1) * 4096;                           \
        if ((u) > 0) GRP(C, 0, MF_PV1, vf1, qf0); else GRP(C, 0, "s_nop 0", qf0, qf0);                                                                \
        SBAR(); vf0 = rd128(rb + so + 2048); H1; SBAR();                                                                                              \
        if ((u) < 31) GRP(C, 2, MF_QK0, kf0, qf0); else GRP(C, 2, "s_nop 0", qf0, qf0);                                                               \
        SBAR(); vf1 = rd128(rb + so + 3072); H2; SBAR();                                                                                              \
        if ((u) < 31) GRP(C, 8, MF_QK1, kf1, qf1); else GRP(C, 8, "s_nop 0", qf0, qf0);                                                               \
        SBAR(); if ((u) < 30) kf0 = rd128(rb + sn); else asm volatile("s_nop 0"); H3; SBAR();                                                         \
        GRP(C, 10, MF_PV0, vf0, qf0);                                                                                                                 \
        SBAR(); if ((u) < 30) kf1 = rd128(rb + sn + 1024); else asm volatile("s_nop 0"); H4; SBAR();                                                  \
    }
#define VM(k, i) vm(IC<(k)>{}, IC<(i)>{})
#define STAGE(s)                                                                                                                                      \
    UNIT(224, 2 * (s), (void)0, (bar(IC<(s) + 1>{}), VM((s) + 1, 0)), (void)0, VM((s) + 1, 1))                                                        \
    UNIT(240, 2 * (s) + 1, VM((s) + 1, 2), (void)0, VM((s) + 1, 3), VM((s) + 1, 4))
        VM(0, 3); VM(0, 4);
        STAGE(0) STAGE(1) STAGE(2) STAGE(3) STAGE(4) STAGE(5) STAGE(6) STAGE(7) STAGE(8) STAGE(9) STAGE(10) STAGE(11) STAGE(12) STAGE(13) STAGE(14) STAGE(15)
        SBAR();
        asm volatile("s_nop 1\n\tv_mfma_f32_32x32x16_bf16 %0, %2, v[248:251], %0\n\ts_nop 15" : "+v"(o), "+{v[240:255]}"(sb) : "v"(vf1));
    }
    STAMP(6);
    if (cls == 1) {
#pragma unroll
        for (int i = 0; i < 4; ++i) *(LAS f32x4*)(smem + L_COMB + t * 4096 + i * 1024 + l16) = f32x4{o[4 * i], o[4 * i + 1], o[4 * i + 2], o[4 * i + 3]};
    }
#pragma unroll
    for (int i = 0; i < 28; i += 4) asm volatile("" : "+v"(hf[i]), "+v"(hf[i + 1]), "+v"(hf[i + 2]), "+v"(hf[i + 3]));
    asm volatile("" : "+v"(g2));
    wait_bar<0>();
    if (cls == 0) {
#pragma unroll
        for (int i = 0; i < 4; ++i) { const f32x4 v = *(const LAS f32x4*)(lds + L_COMB + t * 4096 + i * 1024 + l16); o[4 * i] += v[0]; o[4 * i + 1] += v[1]; o[4 * i + 2] += v[2]; o[4 * i + 3] += v[3]; }
        float l = from_hi(o[12]);
        if (!__all(l > 1e-18f && l < 1e18f)) {
            u32x4 qz = __builtin_bit_cast(u32x4, qf1);
            if (hh) qz[2] = 0;
            const bf16x8 qz1 = __builtin_bit_cast(bf16x8, qz);
            const unsigned kb = WS_KV + head * (64 * 4096);
            float mx = -INFINITY;
#pragma unroll 1
            for (int kt = 0; kt < 64; ++kt) {
                const u32x4 k0 = __builtin_amdgcn_raw_buffer_load_b128(rs, l16, kb + kt * 4096, 16), k1 = __builtin_amdgcn_raw_buffer_load_b128(rs, l16, kb + kt * 4096 + 1024, 16);
                f32x16 s = MFMA(__builtin_bit_cast(bf16x8, k0), qf0, zero16);
                s = MFMA(__builtin_bit_cast(bf16x8, k1), qz1, s);
#pragma unroll
                for (int i = 0; i < 16; ++i) mx = __builtin_fmaxf(mx, s[i]);
            }
            mx = swap_max(mx);
            o = zero16;
#pragma unroll 1
            for (int kt = 0; kt < 64; ++kt) {
                const u32x4 k0 = __builtin_amdgcn_raw_buffer_load_b128(rs, l16, kb + kt * 4096, 16), k1 = __builtin_amdgcn_raw_buffer_load_b128(rs, l16, kb + kt * 4096 + 1024, 16);
                const u32x4 v0 = __builtin_amdgcn_raw_buffer_load_b128(rs, l16, kb + kt * 4096 + 2048, 16), v1 = __builtin_amdgcn_raw_buffer_load_b128(rs, l16, kb + kt * 4096 + 3072, 16);
                f32x16 s = MFMA(__builtin_bit_cast(bf16x8, k0), qf0, zero16);
                s = MFMA(__builtin_bit_cast(bf16x8, k1), qz1, s);
#pragma unroll
                for (int i = 0; i < 16; ++i) s[i] = __builtin_amdgcn_exp2f(s[i] - mx);
                const u32x4 p0 = {pk(s[4], s[0]), pk(s[5], s[1]), pk(s[6], s[2]), pk(s[7], s[3])}, p1 = {pk(s[12], s[8]), pk(s[13], s[9]), pk(s[14], s[10]), pk(s[15], s[11])};
                o = MFMA(__builtin_bit_cast(bf16x8, v0), __builtin_bit_cast(bf16x8, p0), o);
                o = MFMA(__builtin_bit_cast(bf16x8, v1), __builtin_bit_cast(bf16x8, p1), o);
            }
            l = from_hi(o[12]);
        }
        o *= gate * __builtin_amdgcn_rcpf(l);
        if (hh) o[12] = 0.f;
        const u32x4 o0 = {pk(o[0], o[1]), pk(o[2], o[3]), pk(o[4], o[5]), pk(o[6], o[7])}, o1 = {pk(o[8], o[9]), pk(o[10], o[11]), pk(o[12], o[13]), pk(o[14], o[15])};
        st128(ws, WS_O + (tile * 16 + head) * 2048, l16, o0); st128(ws, WS_O + (tile * 16 + head) * 2048 + 1024, l16, o1);
        if (!busy) { wait_vm<0>(); set_flag(ws, WS_FO + (tile * 16 + head) * 8, tag, lane); }
    }
    STAMP(7);
    if (!busy) return;
    const lptr db = lds + (4 * (g16 >> 1) + q4) * 128 + (((2 * (g16 & 1) + (p4 >> 1)) ^ (((q4 >> 1) & 1) << 2)) << 4) + 8 * (p4 & 1);
    const lptr d0 = db + dk, d1 = (lptr)((unsigned)(uintptr_t)d0 ^ 64u), e0 = db + d2, e1 = (lptr)((unsigned)(uintptr_t)e0 ^ 64u);
    auto dma_wo = [&](int half, int e) {
        const int kq_ = 2 * half + cls, pc = 4 * t + e;
        int row = 112 * kq_ + 8 * pc; if (row > 440) row = 440;
        dma<false>(vd, wo + (size_t)row * 896 + fc * 128, L0 + (half ? 48 * K1 + cls * 16384 : 32 * K1 + cls * (112 * K1)) + pc * 1024);
    };
    const unsigned fo_off = WS_FO + (T2 * 16 + 4 * kq + (lane & 3)) * 8;
    u32x4 of[8];
    a0 = zero16; a1 = zero16;
    {
        u64 fo = 0;
        bf16x8 wa = rdtr2(d0, 1024), wb = rdtr2(d1, 1024);
#pragma unroll
        for (int s = 0; s < 28; ++s) {
            const int i = (s + 1) >> 2, e = (s + 1) & 3;
            if (e == 0) {
                if (i == 1) { if (cls == 0) { wait_vm<0>(); set_flag(ws, WS_FO + (tile * 16 + head) * 8, tag, lane); } lds_bar(); }
                else if (i <= 3) lds_bar();
                else if (i == 4) wait_bar<12>();
                else if (i == 5) wait_bar<13>();
                else if (i == 6) { wait_bar<4>(); if (!flags_set(fo, tag)) poll(ws, fo_off, tag); }
                else wait_bar<8>();
            }
            const bf16x8 ca = wa, cb = wb;
            if (s < 27) {
                const int so = (i == 0 || i == 5) ? 0 : (i == 1 || i == 6) ? 112 * K1 : (i == 3) ? 48 * K1 : 80 * K1;
                if (i == 2) { wa = rdtr2(e0 + e * 2048, 1024); wb = rdtr2(e1 + e * 2048, 1024); }
                else { wa = rdtr2(d0 + so + e * 2048, 1024); wb = rdtr2(d1 + so + e * 2048, 1024); }
            }
            a0 = MFMA(ca, __builtin_bit_cast(bf16x8, hf[s]), a0);
            SBAR();
            if (i == 0) { if (cls == 1) { dma_wd(4, 2 + (t >> 1), 4 * (t & 1) + e, 96 * K1 + (t >> 1) * 8192); if (e == 3) dma_wd(4, 2 + (t >> 1), 4 * (t & 1), 96 * K1 + (t >> 1) * 8192); } }
            else if (i == 1) { if (e < 2) dma_wd(4, cls, 2 * t + e, 80 * K1 + cls * 8192); else { dma_wd(5, kq, 4 * tt + 2 * (e - 2), dk); dma_wd(5, kq, 4 * tt + 2 * (e - 2) + 1, dk); } }
            else if (i == 2) dma_wd(6, kq, 4 * tt + e, 112 * K1 + dk);
            else if (i == 3) dma_wo(0, e);
            else if (i == 4) { if (e == 0) fo = flag_load(ws, fo_off); dma_wo(1, e); }
            else if (i == 6) {
                of[2 * e] = __builtin_amdgcn_raw_buffer_load_b128(rs, l16, WS_O + (T2 * 16 + 4 * kq) * 2048 + 2 * e * 1024, 16);
                of[2 * e + 1] = __builtin_amdgcn_raw_buffer_load_b128(rs, l16, WS_O + (T2 * 16 + 4 * kq) * 2048 + (2 * e + 1) * 1024, 16);
            }
            SBAR();
            a1 = MFMA(cb, __builtin_bit_cast(bf16x8, hf[s]), a1);
        }
    }
    STAMP(8);
    {
        const float g1 = 1.f - g2;
        a0 *= g1; a1 *= g1;
        const lptr ob0 = db + (kq == 0 ? 32 * K1 : kq == 1 ? 144 * K1 : 16 * K1 + kq * 16384), ob1 = (lptr)((unsigned)(uintptr_t)ob0 ^ 64u);
#pragma unroll
        for (int e = 0; e < 8; ++e) {
            const int row = 28 * (e >> 1) + 16 * (e & 1);
            const bf16x8 ca = rdtr2(ob0 + row * 128, 1024), cb = rdtr2(ob1 + row * 128, 1024);
            a0 = MFMA(ca, __builtin_bit_cast(bf16x8, of[e]), a0); a1 = MFMA(cb, __builtin_bit_cast(bf16x8, of[e]), a1);
        }
    }
    STAMP(9);
    {
        const unsigned wb_ = L_RED + kq * 16384 + (32 * tt + r31) * 256;
#pragma unroll
        for (int i = 0; i < 4; ++i) {
            *(LAS f32x4*)(smem + wb_ + (((2 * i + hh) ^ (r31 & 7)) << 4)) = f32x4{a0[4 * i], a0[4 * i + 1], a0[4 * i + 2], a0[4 * i + 3]};
            *(LAS f32x4*)(smem + wb_ + (((8 + 2 * i + hh) ^ (r31 & 7)) << 4)) = f32x4{a1[4 * i], a1[4 * i + 1], a1[4 * i + 2], a1[4 * i + 3]};
        }
        lds_bar();
        const int tok = 8 * w + (lane >> 3), c0 = 2 * (lane & 7);
        const lptr rp0 = lds + L_RED + tok * 256 + ((c0 ^ (tok & 7)) << 4), rp1 = lds + L_RED + tok * 256 + (((c0 + 1) ^ (tok & 7)) << 4);
        f32x4 s0 = *(const LAS f32x4*)rp0, s1 = *(const LAS f32x4*)rp1;
#pragma unroll
        for (int k = 1; k < 4; ++k) { s0 += *(const LAS f32x4*)(rp0 + k * 16384); s1 += *(const LAS f32x4*)(rp1 + k * 16384); }
        const u32x4 res = {pk(s0[0], s0[1]), pk(s0[2], s0[3]), pk(s1[0], s1[1]), pk(s1[2], s1[3])};
        *(u32x4*)(out + (size_t)(64 * ft + tok) * 896 + fc * 128 + (lane & 7) * 16) = res;
        if (b == 0 && w == 0) set_flag(ws, WS_EPOCH, tag, lane);
    }
    STAMP(10);
}

extern "C" void kernel_launch(void* const* d_in, const int* in_sizes, int n_in, void* d_out, int out_size, void* d_ws, size_t ws_size, hipStream_t stream) {
    (void)in_sizes; (void)n_in; (void)out_size; (void)ws_size;
    (void)hipFuncSetAttribute((const void*)fused_kernel, hipFuncAttributeMaxDynamicSharedMemorySize, LDS_BYTES);
    fused_kernel<<<256, 512, LDS_BYTES, stream>>>((const char*)d_in[0], (const char*)d_in[1], (const char*)d_in[2], (const char*)d_in[4], (const char*)d_in[5],
                                                  (const char*)d_in[6], (const char*)d_in[7], (char*)d_out, (char*)d_ws);
}
```

```cpp
#include <hip/hip_runtime.h>
#include <hip/hip_bf16.h>
#include <stdint.h>
#include <type_traits>

typedef unsigned long long u64;
using bf16x8 = __attribute__((ext_vector_type(8))) short;
using s16x4 = __attribute__((ext_vector_type(4))) short;
using f32x16 = __attribute__((ext_vector_type(16))) float;
using f32x4 = __attribute__((ext_vector_type(4))) float;
using f32x2 = __attribute__((ext_vector_type(2))) float;
using u32x4 = __attribute__((ext_vector_type(4))) unsigned;
using u32x2 = __attribute__((ext_vector_type(2))) unsigned;
using bf2 = __attribute__((ext_vector_type(2))) __bf16;
#define LAS __attribute__((address_space(3)))
#define GAS __attribute__((address_space(1)))
typedef LAS const char* lptr;
#define DI __device__ __forceinline__
#define MFMA(a, b, c) __builtin_amdgcn_mfma_f32_32x32x16_bf16((a), (b), (c), 0, 0, 0)
#define SBAR() __builtin_amdgcn_sched_barrier(0)

constexpr unsigned WS_EPOCH = 0, WS_G = 0x2000, WS_FW = 0x8000, WS_FKV = 0x10000, WS_FH = 0x18000, WS_WP = 0x40000, WS_KV = 0x1C0000, WS_H = 0x5C0000,
                   WS_O = 0xCC0000, WS_END = 0xEC0000;
constexpr u64 TAG_STEP = 0x9E3779B97F4A7C15ull;
constexpr int LDS_BYTES = 163840;
constexpr unsigned K1 = 1024, L_WP = 0, L_QX = 80 * K1, L_CNT = 88 * K1, L_COMB = 112 * K1, L_RED = 96 * K1;
constexpr unsigned C_KV = 0, C_H = 4, C_CMB = 8, C_FIN = 12, C_Q = 16, C_B = 20, C_GO = 32  ;
constexpr float QSCALE = 0.18898223650461362f * 1.4426950408889634f;
constexpr unsigned EMPTY = 0xFFFFFFFFu;
constexpr unsigned SPIN_CAP = 1u << 19;

template <bool SC1> DI void dma(unsigned voff, const char* sbase, unsigned lds) {
    if (SC1) asm volatile("s_mov_b32 m0, %2\n\ts_nop 0\n\tglobal_load_lds_dwordx4 %0, %1 sc1" ::"v"(voff), "s"(sbase), "s"(lds) : "memory");
    else asm volatile("s_mov_b32 m0, %2\n\ts_nop 0\n\tglobal_load_lds_dwordx4 %0, %1" ::"v"(voff), "s"(sbase), "s"(lds) : "memory");
}
DI u32x4 srd(const void* p) { const u64 a = (u64)p; return u32x4{(unsigned)a, (unsigned)(a >> 32) & 0xffffu, 0x7fffffffu, 0x00020000u}; }
DI void bdma(unsigned voff, u32x4 rsrc, unsigned goff, unsigned lbase, unsigned loff) {
    unsigned so;
    asm volatile("s_add_u32 m0, %3, %4\n\ts_mov_b32 %0, %5\n\tbuffer_load_dwordx4 %1, %2, %0 offen lds" : "=&s"(so) : "v"(voff), "s"(rsrc), "s"(lbase), "i"(loff), "i"(goff) : "memory", "scc");
}
DI void dma2(unsigned voff, const char* sbase, unsigned lds) {
    asm volatile("s_mov_b32 m0, %2\n\ts_nop 0\n\tglobal_load_lds_dwordx4 %0, %1 sc1\n\tglobal_load_lds_dwordx4 %0, %1 offset:1024 sc1" ::"v"(voff), "s"(sbase), "s"(lds) : "memory");
}
template <int N> DI void wait_vm() { asm volatile("s_waitcnt vmcnt(%0)" ::"i"(N) : "memory"); }
template <int N> DI void wait_bar() { asm volatile("s_waitcnt vmcnt(%0) lgkmcnt(0)\n\ts_barrier" ::"i"(N) : "memory"); }
DI void lds_bar() { asm volatile("s_waitcnt lgkmcnt(0)\n\ts_barrier" ::: "memory"); }
DI lptr opq(lptr p) { asm volatile("" : "+v"(p)); return p; }
DI bf16x8 rd128(lptr p) { return *(const LAS bf16x8*)p; }
typedef short v4i16_t __attribute__((ext_vector_type(4)));
DI s16x4 rdtr(lptr p) { return __builtin_bit_cast(s16x4, __builtin_amdgcn_ds_read_tr16_b64_v4i16((LAS v4i16_t*)p)); }
DI bf16x8 rdtr2(lptr p, int d) { const s16x4 a = rdtr(p), b = rdtr(p + d); return __builtin_shufflevector(a, b, 0, 1, 2, 3, 4, 5, 6, 7); }
DI unsigned pk(float lo, float hi) { const f32x2 v = {lo, hi}; return __builtin_bit_cast(unsigned, __builtin_convertvector(v, bf2)); }
DI float swap_max(float v) { auto r = __builtin_amdgcn_permlane32_swap(__float_as_uint(v), __float_as_uint(v), false, false); return __builtin_fmaxf(__uint_as_float(r[0]), __uint_as_float(r[1])); }
DI float from_hi(float v) { auto r = __builtin_amdgcn_permlane32_swap(__float_as_uint(v), __float_as_uint(v), false, false); return __uint_as_float(r[1]); }
DI void st128(char* ws, unsigned off, unsigned voff, u32x4 v) {
    asm volatile("global_store_dwordx4 %0, %1, %2 sc1\n\ts_nop 1" ::"v"(voff), "v"(v), "s"(ws + off) : "memory");
}
DI u64 flag_load(const char* ws, unsigned voff) { u64 v; asm volatile("global_load_dwordx2 %0, %1, %2 sc1" : "=v"(v) : "v"(voff), "s"(ws) : "memory"); return v; }
DI u64 make_tag(u64 ep) {
    unsigned lo, hi; asm volatile("v_readfirstlane_b32 %0, %2\n\tv_readfirstlane_b32 %1, %3\n\ts_nop 0" : "=&s"(lo), "=s"(hi) : "v"((unsigned)ep), "v"((unsigned)(ep >> 32)) : "memory");
    return (((u64)hi << 32) | lo) + TAG_STEP;
}
DI bool flags_set(u64 f, u64 tag) { u64 m; asm volatile("v_cmp_ne_u64 %0, %1, %2\n\ts_nop 0" : "=s"(m) : "s"(tag), "v"(f) : "memory"); return m == 0; }
template <int NAP = 6> DI void poll(const char* ws, unsigned voff, u64 tag) {
#pragma nounroll
    for (unsigned spins = 0; spins < SPIN_CAP; ++spins) {
        u64 v; asm volatile("global_load_dwordx2 %0, %1, %2 sc1\n\ts_waitcnt vmcnt(0)" : "=&v"(v) : "v"(voff), "s"(ws) : "memory");
        if (__all(v == tag)) break;
        __builtin_amdgcn_s_sleep(NAP);
    }
}
DI void set_flag(char* ws, unsigned off, u64 tag, int lane) {
    if (lane == 0) __hip_atomic_store((GAS u64*)(ws + off), tag, __ATOMIC_RELAXED, __HIP_MEMORY_SCOPE_AGENT);
}
template <int I> DI u32x2 geglu4(const f32x16& a) {
    constexpr float C2 = -2.f * 0.7978845608028654f * 1.4426950408889634f * 0.044715f;
    unsigned t0, t1, t2, t3, p0, p1, p2, p3;
    asm("s_nop 11\n\t"
        "v_mul_f32 %0, %8, %8\n\tv_mul_f32 %1, %9, %9\n\tv_mul_f32 %2, %10, %10\n\tv_mul_f32 %3, %11, %11\n\t"
        "v_fmaak_f32 %0, %16, %0, 0xc0135761\n\tv_fmaak_f32 %1, %16, %1, 0xc0135761\n\tv_fmaak_f32 %2, %16, %2, 0xc0135761\n\tv_fmaak_f32 %3, %16, %3, 0xc0135761\n\t"
        "v_mul_f32 %0, %0, %8\n\tv_mul_f32 %1, %1, %9\n\tv_mul_f32 %2, %2, %10\n\tv_mul_f32 %3, %3, %11\n\t"
        "v_exp_f32 %0, %0\n\tv_exp_f32 %1, %1\n\tv_exp_f32 %2, %2\n\tv_exp_f32 %3, %3\n\t"
        "v_mul_f32 %4, %8, %12\n\tv_mul_f32 %5, %9, %13\n\tv_mul_f32 %6, %10, %14\n\tv_mul_f32 %7, %11, %15\n\t"
        "v_add_f32 %0, 1.0, %0\n\tv_add_f32 %1, 1.0, %1\n\tv_add_f32 %2, 1.0, %2\n\tv_add_f32 %3, 1.0, %3\n\t"
        "v_rcp_f32 %0, %0\n\tv_rcp_f32 %1, %1\n\tv_rcp_f32 %2, %2\n\tv_rcp_f32 %3, %3\n\t"
        "v_mul_f32 %4, %4, %0\n\tv_mul_f32 %5, %5, %1\n\tv_mul_f32 %6, %6, %2\n\tv_mul_f32 %7, %7, %3\n\t"
        "v_cvt_pk_bf16_f32 %0, %4, %5\n\tv_cvt_pk_bf16_f32 %1, %6, %7"
        : "=&v"(t0), "=&v"(t1), "=&v"(t2), "=&v"(t3), "=&v"(p0), "=&v"(p1), "=&v"(p2), "=&v"(p3)
        : "v"(a[I]), "v"(a[I + 1]), "v"(a[I + 2]), "v"(a[I + 3]), "v"(a[8 + I]), "v"(a[9 + I]), "v"(a[10 + I]), "v"(a[11 + I]), "v"(C2));
    return u32x2{t0, t1};
}
struct G4 { float t0, t1, t2, t3, p0, p1, p2, p3; };
template <int ST, int I> DI void geglu_step(G4& g, const f32x16& a, float c2, u32x4& out) {
#define T4_ "+v"(g.t0), "+v"(g.t1), "+v"(g.t2), "+v"(g.t3)
#define A4_(o) "v"(a[(o) + I]), "v"(a[(o) + I + 1]), "v"(a[(o) + I + 2]), "v"(a[(o) + I + 3])
    if constexpr (ST == 0) asm volatile("v_mul_f32 %0, %4, %4\n\tv_mul_f32 %1, %5, %5\n\tv_mul_f32 %2, %6, %6\n\tv_mul_f32 %3, %7, %7" : "=&v"(g.t0), "=&v"(g.t1), "=&v"(g.t2), "=&v"(g.t3) : A4_(0));
    if constexpr (ST == 1) asm volatile("v_fmaak_f32 %0, %4, %0, 0xc0135761\n\tv_fmaak_f32 %1, %4, %1, 0xc0135761\n\tv_fmaak_f32 %2, %4, %2, 0xc0135761\n\tv_fmaak_f32 %3, %4, %3, 0xc0135761" : T4_ : "v"(c2));
    if constexpr (ST == 2) asm volatile("v_mul_f32 %0, %0, %4\n\tv_mul_f32 %1, %1, %5\n\tv_mul_f32 %2, %2, %6\n\tv_mul_f32 %3, %3, %7" : T4_ : A4_(0));
    if constexpr (ST == 3) asm volatile("v_exp_f32 %0, %0\n\tv_exp_f32 %1, %1\n\tv_mul_f32 %2, %4, %6\n\tv_mul_f32 %3, %5, %7" : "+v"(g.t0), "+v"(g.t1), "=&v"(g.p0), "=&v"(g.p1) : "v"(a[I]), "v"(a[I + 1]), "v"(a[8 + I]), "v"(a[9 + I]));
    if constexpr (ST == 4) asm volatile("v_exp_f32 %0, %0\n\tv_exp_f32 %1, %1\n\tv_mul_f32 %2, %4, %6\n\tv_mul_f32 %3, %5, %7" : "+v"(g.t2), "+v"(g.t3), "=&v"(g.p2), "=&v"(g.p3) : "v"(a[I + 2]), "v"(a[I + 3]), "v"(a[10 + I]), "v"(a[11 + I]));
    if constexpr (ST == 5) asm volatile("v_add_f32 %0, 1.0, %0\n\tv_add_f32 %1, 1.0, %1\n\tv_add_f32 %2, 1.0, %2\n\tv_add_f32 %3, 1.0, %3" : T4_);
    if constexpr (ST == 6) asm volatile("v_rcp_f32 %0, %0\n\tv_rcp_f32 %1, %1" : "+v"(g.t0), "+v"(g.t1));
    if constexpr (ST == 7) asm volatile("v_rcp_f32 %0, %0\n\tv_rcp_f32 %1, %1\n\tv_mul_f32 %2, %2, %4\n\tv_mul_f32 %3, %3, %5" : "+v"(g.t2), "+v"(g.t3), "+v"(g.p0), "+v"(g.p1) : "v"(g.t0), "v"(g.t1));
    if constexpr (ST == 8) {
        unsigned x_, y_;
        asm volatile("v_mul_f32 %2, %2, %6\n\tv_mul_f32 %3, %3, %7\n\tv_cvt_pk_bf16_f32 %0, %4, %5\n\tv_cvt_pk_bf16_f32 %1, %2, %3" : "=&v"(x_), "=&v"(y_), "+v"(g.p2), "+v"(g.p3) : "v"(g.p0), "v"(g.p1), "v"(g.t2), "v"(g.t3));
        out[I / 2] = x_; out[I / 2 + 1] = y_;
    }
#undef T4_
#undef A4_
}
DI u32x4 geglu_pk(const f32x16& a) {
    constexpr float C1 = -2.f * 0.7978845608028654f * 1.4426950408889634f, C2 = C1 * 0.044715f;
    u32x4 o;
#pragma unroll
    for (int i = 0; i < 4; ++i) {
        const f32x2 g = {a[2 * i], a[2 * i + 1]}, u = {a[8 + 2 * i], a[9 + 2 * i]};
        const f32x2 t = g * g * C2 + C1, z = t * g;
        const f32x2 d = f32x2{__builtin_amdgcn_exp2f(z[0]), __builtin_amdgcn_exp2f(z[1])} + 1.f;
        const f32x2 h = g * u * f32x2{__builtin_amdgcn_rcpf(d[0]), __builtin_amdgcn_rcpf(d[1])};
        o[i] = pk(h[0], h[1]);
    }
    return o;
}
DI u32x4 geglu(const f32x16& a) { const u32x2 x = geglu4<0>(a), y = geglu4<4>(a); return u32x4{x[0], x[1], y[0], y[1]}; }

constexpr int GO_UNIT = 63, HLA = 18, PAUSE = 10;
template <int N> using IC = std::integral_constant<int, N>;
template <int I, int N, class F> DI void static_for(F&& f) { if constexpr (I < N) { f(IC<I>{}); static_for<I + 1, N>(f); } }

__global__ __launch_bounds__(512, 2) void fused_kernel(const char* __restrict__ x, const char* __restrict__ wqkv, const char* __restrict__ wo, const char* __restrict__ wg,
                                                       const char* __restrict__ wu, const char* __restrict__ wd, const char* __restrict__ gw, char* __restrict__ out, char* ws) {
    extern __shared__ __attribute__((aligned(16))) char smem[];
    const int tid = threadIdx.x, lane = tid & 63, w = __builtin_amdgcn_readfirstlane(tid >> 6), t = w & 3, cls = w >> 2;
    const int r31 = lane & 31, hh = lane >> 5, g16 = lane >> 4, q4 = (lane >> 2) & 3, p4 = lane & 3;
    const int b = blockIdx.x, x8 = b & 7, j = b >> 3, hg = x8 & 3, head = 4 * hg + (j & 3), r = 8 * (x8 >> 2) + (j >> 2);
    const bool busy = head < 14;
    const int fi = (x8 >> 2) * 112 + hg * 32 + (hg < 3 ? j : (j >> 2) * 2 + (j & 1)), ft = fi / 7, fc = fi - 7 * ft;
    const unsigned L0 = (unsigned)(uintptr_t)smem;
    const lptr lds = (lptr)smem;
    const __amdgpu_buffer_rsrc_t rs = __builtin_amdgcn_make_buffer_rsrc(ws, 0, WS_END, 0x00020000);
    const unsigned l16 = lane * 16;
    const f32x16 zero16 = {};

    const unsigned vx0 = (lane >> 3) * 896 + (((lane & 7) ^ g16) << 4), vx1 = (8 + (lane >> 3)) * 896 + (((lane & 7) ^ (4 + g16)) << 4);
    const unsigned csw = (lane & 7) ^ ((g16 & 1) << 2);
    const unsigned vg = (lane >> 3) * 3584 + (csw << 4), vu = (lane >> 3) * 3584 + ((csw ^ 2) << 4), vd = (lane >> 3) * 896 + (csw << 4);
    const char* xs = x + (size_t)(128 * r + 16 * w) * 896;
    const char* wgs = wg + (8 * w) * 3584 + head * 256;
    const char* wus = wu + (8 * w) * 3584 + head * 256;
    const u32x4 rx = srd(xs), rg_ = srd(wgs), ru_ = srd(wus);
    const unsigned lw1 = L0 + 1024 * w, lw2 = L0 + 2048 * w;
    auto dma_x = [&](int i, int e, unsigned dst) __attribute__((always_inline)) { bdma(e ? vx1 : vx0, rx, 128 * i, lw2, dst + 1024 * e); };
    auto dma_gu = [&](int i, int pass, int e, unsigned dst) __attribute__((always_inline)) { bdma(e ? vu : vg, e ? ru_ : rg_, i * (64 * 3584) + pass * 128, lw1, dst + 8192 * e); };
    auto dma_pair = [&](int i, int e) { if (e < 2) dma_x(i, e, (i % 5) * 32768); else dma_gu(i, 0, e - 2, (i % 5) * 32768 + 16384); };
    auto gub_at = [](int i) { return i < 2 ? 128 * K1 + i * 16384 : i < 4 ? 96 * K1 + (i - 2) * 16384 : (i - 4) * 16384; };
    auto dma_wp = [&]() {
        const unsigned o_ = cls * 49152 + t * 2048;
        const char* src = ws + WS_WP + head * (84 * 1024) + o_;
#pragma unroll
        for (int m = 0; m < 4; ++m) dma2(l16, src + m * 8192, L0 + L_WP + o_ + m * 8192);
        if (cls == 0) { dma2(l16, src + 4 * 8192, L0 + L_WP + o_ + 4 * 8192); dma2(l16, src + 5 * 8192, L0 + L_WP + o_ + 5 * 8192); }
        else dma<true>(l16, src + 32768 - t * 1024, L0 + L_WP + o_ + 32768 - t * 1024);
    };
    const unsigned xm = (r31 >> 1) & 7;
    const lptr xb = lds + (32 * t + r31) * 128;
    const lptr gub = lds + (g16 & 1) * 8192 + (8 * (g16 >> 1) + q4) * 128 + (((4 * cls + (p4 >> 1)) ^ (((q4 >> 1) & 1) << 2) ^ ((g16 & 1) << 1)) << 4) + 8 * (p4 & 1);
    const lptr gub1 = (lptr)((unsigned)(uintptr_t)gub ^ 32u);
    const unsigned fw_off = WS_FW + x8 * 4096 + (lane < 28 ? lane : 0) * 128;

    bf16x8 xf[28];
    f32x16 a0, a1;
    u32x4 h0, h1;
    unsigned touch2 = 0;
    u64 tag;
    const int tile = 4 * r + t;
    const unsigned kv_off = WS_KV + (head * 64 + tile) * 4096;
    const unsigned h_off = WS_H + (tile * 112 + head * 8 + 2 * cls) * 1024;
    if (__builtin_expect(busy, 1)) {
#pragma unroll
        for (int e = 0; e < 4; ++e) dma_pair(0, e);
        const u64 ep = flag_load(ws, WS_EPOCH);
        asm volatile("s_barrier" ::: "memory");
#pragma unroll
        for (int e = 0; e < 4; ++e) dma_pair(1, e);
        unsigned touch;
        const int wl = (hg < 3 ? j : (j >> 2) * 2 + (j & 1)) * 8 + w, sh = hg < 3 ? 2 : 1;
        {
            const int per = hg < 3 ? 30 : 50, id = wl * per + (lane < per ? lane : per - 1);
            const int row = (id * 52429) >> 18, q = id - 5120;
            const char* a = id < 5120 ? x + (1024 * (x8 >> 2) + row) * 896 + (2 + id - 5 * row) * 128
                                      : (((q >> sh) & 1) ? wu : wg) + (128 + (q >> (sh + 1))) * 3584 + (4 * hg + (q & ((1 << sh) - 1))) * 256;
            u64 pc; asm volatile("s_getpc_b64 %0" : "=s"(pc));
            if (lane >= 62) a = (const char*)(pc & ~127ull) + ((wl & 127) * 2 + lane - 62) * 120;
            asm volatile("global_load_dword %0, %1, off" : "=v"(touch) : "v"(a) : "memory");
        }
        lptr g0[3], g1[3], xr[3][4];
#pragma unroll
        for (int i = 0; i < 3; ++i) {
            g0[i] = opq(gub + 16384 + i * 65536); g1[i] = opq(gub1 + 16384 + i * 65536);
#pragma unroll
            for (int k = 0; k < 4; ++k) xr[i][k] = opq(xb + (((2 * k + hh) ^ xm) << 4) + i * 65536);
        }
        bf16x8 wa[3], wb[3];
        auto rdA = [&](int n) {
            const int sl = (n >> 2) % 5, ks = n & 3, o_ = (sl & 1) * 32768;
            wa[n % 3] = rdtr2(g0[sl >> 1] + o_ + ks * 2048, 512); xf[n] = rd128(xr[sl >> 1][ks] + o_);
        };
        auto rdB = [&](int n) { const int sl = (n >> 2) % 5; wb[n % 3] = rdtr2(g1[sl >> 1] + (sl & 1) * 32768 + (n & 3) * 2048, 512); };
        wait_bar<5>();
        tag = make_tag(ep);
        u64 fw = 0;
        rdA(0); rdB(0); rdA(1); rdB(1);
        SBAR();
#pragma unroll
        for (int s = 0; s < 28; ++s) {
            if (s == 2) { wait_bar<4>(); asm volatile("" ::"v"(touch)); } else if (s == 10) wait_bar<12>(); else if (s == 14 || s == 18) wait_bar<9>(); else if (s == 6 || s == 22) wait_bar<8>();
            a0 = MFMA(wa[s % 3], xf[s], s ? a0 : zero16);
            asm volatile("" : "+v"(a0));
            SBAR();
            if (s < 26) rdA(s + 2);
            if (s < 10) dma_pair(2 + (s >> 1), 2 * (s & 1));
            SBAR();
            a1 = MFMA(wb[s % 3], xf[s], s ? a1 : zero16);
            asm volatile("" : "+v"(a1));
            SBAR();
            if (s < 26) rdB(s + 2);
            if (s < 10) dma_pair(2 + (s >> 1), 2 * (s & 1) + 1);
            else if (s == 10) {
                const int q = wl * 6 + (lane < 6 ? lane : 5);
                const char* a = (((q >> sh) & 1) ? wu : wg) + (256 + (q >> (sh + 1))) * 3584 + (4 * hg + (q & ((1 << sh) - 1))) * 256 + 128;
                asm volatile("global_load_dword %0, %1, off" : "=v"(touch2) : "v"(a) : "memory");
            }
            else if (s >= 14 && s < 22) dma_gu(((s - 14) >> 1) ^ 2, 1, s & 1, gub_at(((s - 14) >> 1) ^ 2));
            else if (s == 24) { asm volatile("" : "+v"(a0), "+v"(a1)); if (w == 7) fw = flag_load(ws, fw_off); }
            if (s == 22) asm volatile("" ::"v"(touch2));
            SBAR();
        }
        asm volatile("" : "+v"(a0), "+v"(a1));
        if (w == 7) { wait_vm<0>(); if (!flags_set(fw, tag)) poll(ws, fw_off, tag); }
        lds_bar();
        if (cls == 0) dma_wp();
        h0 = geglu_pk(a0); h1 = geglu_pk(a1);
        asm volatile("" : "+v"(h0), "+v"(h1));
    } else {
        const int sp = (head - 14) + 2 * r;
        if (sp < 28) {
            const char* src = wqkv + (size_t)sp * 43008;
#pragma unroll
            for (int m = 0; m < 6; ++m) if (m < 5 || w < 2) dma<false>(l16, src + (m * 8 + w) * 1024, L0 + 112 * K1 + (m * 8 + w) * 1024);
            const u64 ep = flag_load(ws, WS_EPOCH);
            const u32x4 gwf = *(const u32x4*)(gw + 32 * sp + 16 * hh);
            const lptr sb = lds + 112 * K1 + (8 * (g16 >> 1) + q4) * 2688 + (16 * (g16 & 1) + 4 * p4) * 2 + w * 112;
            char* dst = ws + WS_WP + (2 * w * 28 + sp) * 3072;
            wait_bar<0>();
            u32x4 f[6];
#pragma unroll
            for (int e = 0; e < 6; ++e) f[e] = __builtin_bit_cast(u32x4, rdtr2(sb + (e % 3) * 896 + (e / 3) * 56, 4 * 2688));
#pragma unroll
            for (int e = 0; e < 6; ++e) {
                if (r31 >= 28) f[e] = (e % 3 == 0 && r31 == 28) ? gwf : u32x4{0, 0, 0, 0};
                st128(dst, (e / 3) * (84 * 1024) + (e % 3) * 1024, l16, f[e]);
            }
            tag = make_tag(ep);
            wait_bar<0>();
            if (w == 0 && lane < 8) __hip_atomic_store((GAS u64*)(ws + WS_FW + lane * 4096 + sp * 128), tag, __ATOMIC_RELAXED, __HIP_MEMORY_SCOPE_AGENT);
        } else {
            const u64 ep = flag_load(ws, WS_EPOCH);
            wait_vm<0>();
            tag = make_tag(ep);
        }
#pragma unroll
        for (int e = 0; e < 14; ++e) dma_x(e >> 1, e & 1, (e >> 1) * 16384);
        wait_bar<0>();
#pragma unroll
        for (int s = 0; s < 28; ++s) xf[s] = rd128(xb + (s >> 2) * 16384 + (((2 * (s & 3) + hh) ^ xm) << 4));
        if (w == 7) poll(ws, fw_off, tag);
        lds_bar();
        if (cls == 0) dma_wp();
        a0 = zero16; a1 = zero16; h0 = u32x4{}; h1 = h0;
    }
    const lptr wp0 = opq(lds + L_WP + l16), wp1 = opq(lds + L_WP + l16 + 14 * 3072);
    auto wpf = [&](int s, int mat) { return rd128((s < 14 ? wp0 : wp1) + (s < 14 ? s : s - 14) * 3072 + mat * 1024); };
    auto arrive = [&](unsigned c) __attribute__((always_inline)) {
        unsigned n = 0;
        if (lane == 0) n = __hip_atomic_fetch_add((LAS unsigned*)(smem + L_CNT) + (c >> 2), 1u, __ATOMIC_RELAXED, __HIP_MEMORY_SCOPE_WORKGROUP);
        return (unsigned)__builtin_amdgcn_readfirstlane(n);
    };
    auto await = [&](unsigned c, unsigned n, bool patient = false) __attribute__((always_inline)) {
#pragma nounroll
        for (unsigned spins = 0; spins < SPIN_CAP * 16 && ((volatile LAS unsigned*)(smem + L_CNT))[c >> 2] < n; ++spins) {
            if (patient) __builtin_amdgcn_s_sleep(8); else __builtin_amdgcn_s_sleep(1);
        }
        asm volatile("" ::: "memory");
    };
    auto kv_done = [&]() __attribute__((always_inline)) { wait_vm<0>(); if (arrive(C_KV) == 7) set_flag(ws, WS_FKV + (head * 16 + r) * 128, tag, lane); };
    auto h_done = [&]() __attribute__((always_inline)) { if (arrive(C_H) == 7) set_flag(ws, WS_FH + (r * 16 + head) * 128, tag, lane); };
    auto finish = [&]() __attribute__((always_inline)) {
        lds_bar();
        const int tok = 8 * w + (lane >> 3), c0_ = 2 * (lane & 7);
        const lptr rp0 = lds + L_RED + tok * 256 + ((c0_ ^ (tok & 7)) << 4), rp1 = lds + L_RED + tok * 256 + (((c0_ + 1) ^ (tok & 7)) << 4);
        f32x4 s0 = *(const LAS f32x4*)rp0, s1 = *(const LAS f32x4*)rp1;
#pragma unroll
        for (int k = 1; k < 4; ++k) { s0 += *(const LAS f32x4*)(rp0 + k * 16384); s1 += *(const LAS f32x4*)(rp1 + k * 16384); }
        const u32x4 res = {pk(s0[0], s0[1]), pk(s0[2], s0[3]), pk(s1[0], s1[1]), pk(s1[2], s1[3])};
        st128(out, 64 * ft * 896 + fc * 128, tok * 896 + (lane & 7) * 16, res);
        if (b == 0 && w == 0) set_flag(ws, WS_EPOCH, tag, lane);
    };
    auto mark_o = [&]() { st128(ws, WS_O + (tile * 16 + head) * 2048, l16, u32x4{EMPTY, EMPTY, EMPTY, EMPTY}); st128(ws, WS_O + (tile * 16 + head) * 2048 + 1024, l16, u32x4{EMPTY, EMPTY, EMPTY, EMPTY}); };
    if (cls == 0) {
        wait_bar<0>();
        f32x16 aq, ak;
        bf16x8 fq[3], fk[3];
        fq[0] = wpf(0, 0); fk[0] = wpf(0, 1); fq[1] = wpf(1, 0); fk[1] = wpf(1, 1);
        SBAR();
#pragma unroll
        for (int s = 0; s < 28; ++s) {
            if (s == 14) { lds_bar(); if (w == 0 && lane < 16) *(LAS unsigned*)(smem + L_CNT + 4 * lane) = 0; }
            aq = MFMA(fq[s % 3], xf[s], s ? aq : zero16);
            SBAR();
            if (s < 26) fq[(s + 2) % 3] = wpf(s + 2, 0);
            SBAR();
            ak = MFMA(fk[s % 3], xf[s], s ? ak : zero16);
            SBAR();
            if (s < 26) fk[(s + 2) % 3] = wpf(s + 2, 1);
            if (s == 6 && busy) st128(ws, h_off, l16, h0);
            if (s == 8 && busy) st128(ws, h_off + 1024, l16, h1);
            if (s >= 14 && s < 20 && busy) dma_gu(4 + (s - 14) / 2, 1, s & 1, gub_at(4 + (s - 14) / 2));
            SBAR();
        }
        wait_bar<0>();
        if (hh) ak[12] = 1.f;
        const u32x4 k0 = {pk(ak[0], ak[1]), pk(ak[2], ak[3]), pk(ak[4], ak[5]), pk(ak[6], ak[7])}, k1 = {pk(ak[8], ak[9]), pk(ak[10], ak[11]), pk(ak[12], ak[13]), pk(ak[14], ak[15])};
        st128(ws, kv_off, l16, k0); st128(ws, kv_off + 1024, l16, k1);
        const float logit = from_hi(aq[12]);
        const float gate = __builtin_amdgcn_rcpf(1.f + __builtin_amdgcn_exp2f(-1.4426950408889634f * logit));
        if (head == 0) asm volatile("global_store_dword %0, %1, %2 sc1" ::"v"(r31 * 4), "v"(gate), "s"(ws + WS_G + tile * 128) : "memory");
        aq *= QSCALE;
        if (hh) aq[12] = 0.f;
        const u32x4 q0 = {pk(aq[0], aq[1]), pk(aq[2], aq[3]), pk(aq[4], aq[5]), pk(aq[6], aq[7])};
        u32x4 q1 = {pk(aq[8], aq[9]), pk(aq[10], aq[11]), pk(aq[12], aq[13]), pk(aq[14], aq[15])};
        f32x16 sc = MFMA(__builtin_bit_cast(bf16x8, k0), __builtin_bit_cast(bf16x8, q0), zero16);
        sc = MFMA(__builtin_bit_cast(bf16x8, k1), __builtin_bit_cast(bf16x8, q1), sc);
        float m = sc[0];
#pragma unroll
        for (int i = 1; i < 16; ++i) m = __builtin_fmaxf(m, sc[i]);
        m = swap_max(m);
        if (hh) q1[2] = pk(-m, 0.f);
        *(LAS u32x4*)(smem + L_QX + t * 2048 + l16) = q0; *(LAS u32x4*)(smem + L_QX + t * 2048 + 1024 + l16) = q1;
        asm volatile("s_waitcnt lgkmcnt(0)" ::: "memory");
        arrive(C_Q);
        kv_done();
        await(C_Q, 4);
        bf16x8 qf[4][2];
#pragma unroll
        for (int i = 0; i < 8; ++i) qf[i >> 1][i & 1] = rd128(lds + L_QX + ((t + 1 + (i >> 1)) & 3) * 2048 + (i & 1) * 1024 + l16);
        asm volatile("s_waitcnt lgkmcnt(0)" ::: "memory");
        arrive(C_B);
        const unsigned fkv_off = WS_FKV + (head * 16 + 4 * t + (lane & 3)) * 128;
        poll<1>(ws, WS_FKV + (head * 16 + 4 * t) * 128, tag);
        asm volatile("s_waitcnt lgkmcnt(0)" ::: "memory");
        h_done();
        f32x16 o[4] = {zero16, zero16, zero16, zero16};
        auto put = [&](int jq) __attribute__((always_inline)) {
            const unsigned slot = L_COMB + (((t + 1 + jq) & 3) * 3 + jq) * 4096 + l16;
#pragma unroll
            for (int i = 0; i < 4; ++i) *(LAS f32x4*)(smem + slot + i * 1024) = f32x4{o[jq][4 * i], o[jq][4 * i + 1], o[jq][4 * i + 2], o[jq][4 * i + 3]};
        };
        {
            const unsigned kb = WS_KV + (head * 64 + 16 * t) * 4096;
            bf16x8 kv[3][4];
            auto ld = [&](int kt, int f) { kv[kt % 3][f] = __builtin_bit_cast(bf16x8, __builtin_amdgcn_raw_buffer_load_b128(rs, l16 + f * 1024, kb + kt * 4096, 16)); };
#pragma unroll
            for (int i = 0; i < 10; ++i) ld(i >> 2, i & 3);
            u64 fkv = 0;
            f32x16 sa = MFMA(kv[0][0], qf[0][0], zero16), sb;
            sa = MFMA(kv[0][1], qf[0][1], sa);
            sb = zero16;
            SBAR();
            asm volatile("s_nop 15" ::: "memory");
#define SDWA_LO " dst_sel:WORD_0 dst_unused:UNUSED_PRESERVE src0_sel:WORD_1\n\t"
#define EX_(k) "v_exp_f32 v[%c10+" #k "], v[%c10+" #k "]\n\t"
#define PK_(a, b) "v_mov_b32_sdwa v[%c10+" #a "], v[%c10+" #b "]" SDWA_LO
#define MF_PV1 "v_mfma_f32_32x32x16_bf16 %2, %4, v[%c11+8:%c11+11], %2\n\t"
#define MF_QK0 "v_mfma_f32_32x32x16_bf16 v[%c11:%c11+15], %5, %8, 0\n\t"
#define MF_QK1 "v_mfma_f32_32x32x16_bf16 v[%c11:%c11+15], %6, %9, v[%c11:%c11+15]\n\t"
#define MF_PV0 "v_mfma_f32_32x32x16_bf16 %3, %7, v[%c10:%c10+3], %3\n\t"
#define UNIT_(M1, M2, M3)                                                                                                                             \
    asm volatile(EX_(0) EX_(4) M1 EX_(1) EX_(5) PK_(0, 4) PK_(1, 5) M2 EX_(2) EX_(6) EX_(3) EX_(7) PK_(2, 6) PK_(3, 7)                                \
                 M3 EX_(8) EX_(12) EX_(9) EX_(13) PK_(8, 12) PK_(9, 13) MF_PV0 EX_(10) EX_(14) EX_(11) EX_(15) PK_(10, 14) PK_(11, 15)                 \
                 : "+{v[224:239]}"(sa), "+{v[240:255]}"(sb), "+v"(o[jp]), "+v"(o[jq])                                                                 \
                 : "v"(kv[kp % 3][3]), "v"(kv[kn % 3][0]), "v"(kv[kn % 3][1]), "v"(kv[kt % 3][2]), "v"(qf[jn][0]), "v"(qf[jn][1]), "i"(C), "i"(464 - C))
            static_for<0, 64>([&](auto uc) {
                constexpr int u = decltype(uc)::value, kt = u >> 2, jq = u & 3, C = (u & 1) ? 240 : 224;
                constexpr int kn = u < 63 ? (u + 1) >> 2 : 15, jn = (u + 1) & 3, kp = u > 0 ? (u - 1) >> 2 : 0, jp = (u + 3) & 3;
                if constexpr (u == 0) UNIT_("", MF_QK0, MF_QK1); else if constexpr (u == 63) UNIT_(MF_PV1, "", ""); else UNIT_(MF_PV1, MF_QK0, MF_QK1);
                SBAR();
                if constexpr (u == 1) fkv = flag_load(ws, fkv_off);
                if constexpr (u == 6) { wait_vm<4>(); if (!flags_set(fkv, tag)) poll(ws, fkv_off, tag); }
                if constexpr (jq < 2 && kt + 2 < 16) ld(kt + 2, 2 + jq);
                if constexpr (jq >= 2 && kt + 3 < 16) ld(kt + 3, jq - 2);
                if constexpr (u == GO_UNIT) ((volatile LAS unsigned*)(smem + L_CNT + C_GO))[t] = 1;
                if constexpr (u == 56) await(busy ? C_H : C_KV, 8);
                if constexpr (u >= 61) put(u - 61);
                SBAR();
            });
            SBAR();
            asm volatile("s_nop 1\n\tv_mfma_f32_32x32x16_bf16 %0, %2, v[248:251], %0\n\ts_nop 15" : "+v"(o[3]), "+{v[240:255]}"(sb) : "v"(kv[0][3]));
        }
        __builtin_amdgcn_s_setprio(2);
        f32x16 oo = o[3];
        const bf16x8 qa = qf[3][0], qb = qf[3][1];
        asm volatile("s_waitcnt lgkmcnt(0)" ::: "memory");
        arrive(C_CMB);
        await(C_CMB, 4);
        {
            f32x4 pv[12];
#pragma unroll
            for (int i = 0; i < 12; ++i) pv[i] = *(const LAS f32x4*)(lds + L_COMB + t * 12288 + ((i % 3) * 4 + 3 - i / 3) * 1024 + l16);
            SBAR();
#pragma unroll
            for (int i = 0; i < 12; ++i) {
                const int qd = 4 * (3 - i / 3);
                asm volatile("v_add_f32 %0, %0, %4\n\tv_add_f32 %1, %1, %5\n\tv_add_f32 %2, %2, %6\n\tv_add_f32 %3, %3, %7"
                             : "+v"(oo[qd]), "+v"(oo[qd + 1]), "+v"(oo[qd + 2]), "+v"(oo[qd + 3]) : "v"(pv[i][0]), "v"(pv[i][1]), "v"(pv[i][2]), "v"(pv[i][3]));
            }
        }
        u32x4 o0, o1;
        auto scale_pack = [&](const f32x16& a_, float l_) __attribute__((always_inline)) {
            const float f_ = gate * __builtin_amdgcn_rcpf(l_);
            float y[16];
#pragma unroll
            for (int i = 0; i < 16; i += 4)
                asm volatile("v_mul_f32 %0, %4, %8\n\tv_mul_f32 %1, %5, %8\n\tv_mul_f32 %2, %6, %8\n\tv_mul_f32 %3, %7, %8" : "=&v"(y[i]), "=&v"(y[i + 1]), "=&v"(y[i + 2]), "=&v"(y[i + 3])
                             : "v"(a_[i]), "v"(a_[i + 1]), "v"(a_[i + 2]), "v"(a_[i + 3]), "v"(f_));
            if (hh) y[12] = 0.f;
            o0 = u32x4{pk(y[0], y[1]), pk(y[2], y[3]), pk(y[4], y[5]), pk(y[6], y[7])}; o1 = u32x4{pk(y[8], y[9]), pk(y[10], y[11]), pk(y[12], y[13]), pk(y[14], y[15])};
#pragma unroll
            for (int i = 0; i < 4; ++i) { o0[i] = min(o0[i], EMPTY - 1); o1[i] = min(o1[i], EMPTY - 1); }
            asm volatile("" : "+v"(o0), "+v"(o1));
        };
        const float l = from_hi(oo[12]);
        scale_pack(oo, l);
        if (__builtin_expect(!__all(l > 1e-18f && l < 1e18f), 0)) {
            u32x4 qz = __builtin_bit_cast(u32x4, qb);
            if (hh) qz[2] = 0;
            const bf16x8 qz1 = __builtin_bit_cast(bf16x8, qz);
            const unsigned kb = WS_KV + head * (64 * 4096);
            poll(ws, WS_FKV + (head * 16 + (lane & 15)) * 128, tag);
            float mx = -INFINITY;
#pragma unroll 1
            for (int kt = 0; kt < 64; ++kt) {
                const u32x4 k0_ = __builtin_amdgcn_raw_buffer_load_b128(rs, l16, kb + kt * 4096, 16), k1_ = __builtin_amdgcn_raw_buffer_load_b128(rs, l16, kb + kt * 4096 + 1024, 16);
                f32x16 s = MFMA(__builtin_bit_cast(bf16x8, k0_), qa, zero16);
                s = MFMA(__builtin_bit_cast(bf16x8, k1_), qz1, s);
#pragma unroll
                for (int i = 0; i < 16; ++i) mx = __builtin_fmaxf(mx, s[i]);
            }
            mx = swap_max(mx);
            oo = zero16;
#pragma unroll 1
            for (int kt = 0; kt < 64; ++kt) {
                const u32x4 k0_ = __builtin_amdgcn_raw_buffer_load_b128(rs, l16, kb + kt * 4096, 16), k1_ = __builtin_amdgcn_raw_buffer_load_b128(rs, l16, kb + kt * 4096 + 1024, 16);
                const u32x4 v0 = __builtin_amdgcn_raw_buffer_load_b128(rs, l16, kb + kt * 4096 + 2048, 16), v1 = __builtin_amdgcn_raw_buffer_load_b128(rs, l16, kb + kt * 4096 + 3072, 16);
                f32x16 s = MFMA(__builtin_bit_cast(bf16x8, k0_), qa, zero16);
                s = MFMA(__builtin_bit_cast(bf16x8, k1_), qz1, s);
#pragma unroll
                for (int i = 0; i < 16; ++i) s[i] = __builtin_amdgcn_exp2f(s[i] - mx);
                const u32x4 p0 = {pk(s[4], s[0]), pk(s[5], s[1]), pk(s[6], s[2]), pk(s[7], s[3])}, p1 = {pk(s[12], s[8]), pk(s[13], s[9]), pk(s[14], s[10]), pk(s[15], s[11])};
                oo = MFMA(__builtin_bit_cast(bf16x8, v0), __builtin_bit_cast(bf16x8, p0), oo);
                oo = MFMA(__builtin_bit_cast(bf16x8, v1), __builtin_bit_cast(bf16x8, p1), oo);
            }
            asm volatile("s_nop 15" : "+v"(oo));
            scale_pack(oo, from_hi(oo[12]));
        }
        st128(ws, WS_O + (tile * 16 + head) * 2048, l16, o0); st128(ws, WS_O + (tile * 16 + head) * 2048 + 1024, l16, o1);
        __builtin_amdgcn_s_setprio(0);
        arrive(C_FIN);
        if (busy) finish();
#pragma unroll
        for (int i = 0; i < 28; i += 4) asm volatile("" : "=v"(xf[i]), "=v"(xf[i + 1]), "=v"(xf[i + 2]), "=v"(xf[i + 3]));
        asm volatile("" : "=v"(h0), "=v"(h1));
    }
    int cls2 = cls;
    asm volatile("" : "+s"(cls2));
    if (cls2 == 1) {
        f32x16 c0, c1;
        lds_bar();
        f32x16 av;
        bf16x8 fv[4];
        fv[0] = wpf(0, 2); fv[1] = wpf(1, 2); fv[2] = wpf(2, 2);
        dma_wp();
        if (busy) { st128(ws, h_off, l16, h0); st128(ws, h_off + 1024, l16, h1); }
        SBAR();
#pragma unroll
        for (int s = 0; s < 28; ++s) {
            if (s == 13) { if (busy) wait_bar<2>(); else wait_bar<0>(); }
            av = MFMA(xf[s], fv[s & 3], s ? av : zero16);
            SBAR();
            if (s < 25) fv[(s + 3) & 3] = wpf(s + 3, 2);
            if (s >= 14 && s < 26 && !(s & 1) && busy) dma_gu(4 + (s - 14) / 4, 1, (s >> 1) & 1, gub_at(4 + (s - 14) / 4));
            SBAR();
        }
        lptr gb[2][4];
#pragma unroll
        for (int n = 0; n < 4; ++n) { gb[0][n] = opq((lptr)((unsigned)(uintptr_t)gub ^ (unsigned)(64 ^ (n << 5)))); gb[1][n] = opq(gb[0][n] + 96 * K1); }
        bf16x8 wf[8];
        auto rd = [&](int m) {
            const int n = (m % 56) >> 1, hi = n < 16;
            wf[m & 7] = rdtr2(gb[hi][2 * (m / 56) + (m & 1)] + gub_at(n >> 2) - (hi ? 96 * K1 : 0) + (n & 3) * 2048, 512);
        };
#pragma unroll
        for (int m = 0; m < 6; ++m) rd(m ^ 1);
        asm volatile("" : "+v"(av));
        wait_bar<0>();
        u32x4 v0, v1;
        auto v_pack = [&](int i) __attribute__((always_inline)) {
            if (i < 4) { v0[i] = r31 == 28 ? 0x3F803F80u : pk(av[4 + i], av[i]); asm volatile("" : "+v"(v0[i])); }
            else { v1[i - 4] = r31 == 28 ? 0x3F803F80u : pk(av[8 + i], av[4 + i]); asm volatile("" : "+v"(v1[i - 4])); }
        };
        if (!busy) {
#pragma unroll
            for (int i = 0; i < 8; ++i) v_pack(i);
            st128(ws, kv_off + 2048, l16, v0); st128(ws, kv_off + 3072, l16, v1);
            mark_o();
            kv_done();
            const int idx = (((head - 14) + 2 * r) * 4 + t) * 128 + lane;
            const char* a = idx < 12544 ? wd + idx * 128 : wo + (idx < 15680 ? idx - 12544 : 0) * 128;
            const char* a2 = idx + 64 < 12544 ? wd + (idx + 64) * 128 : wo + (idx + 64 < 15680 ? idx + 64 - 12544 : 0) * 128;
            unsigned d0_, d1_; asm volatile("global_load_dword %0, %2, off\n\tglobal_load_dword %1, %3, off\n\ts_waitcnt vmcnt(0)" : "=&v"(d0_), "=&v"(d1_) : "v"(a), "v"(a2) : "memory");
        } else {
        const int kq = t;
        const unsigned rg0 = kq < 3 ? kq * 24 * K1 : 96 * K1, rg1 = kq < 3 ? rg0 + 8 * K1 : 72 * K1;
        const char* wds = wd + (size_t)(448 * kq) * 896 + fc * 128;
        const u32x4 rd_ = srd(wds), ro_ = srd(wo + (size_t)(112 * kq) * 896 + fc * 128);
        const unsigned lr0 = L0 + rg0, lr1 = L0 + rg1;
        auto dma_wd = [&](int st, int pc) __attribute__((always_inline)) { bdma(vd, rd_, (64 * st + 8 * pc) * 896, st % 3 == 0 ? lr0 : lr1, (st % 3 == 2 ? 8192 : 0) + pc * 1024); };
        auto dma_wo = [&](int pc) __attribute__((always_inline)) { bdma(vd, ro_, 8 * (pc < 14 ? pc : 13) * 896, lr1, pc * 1024); };
        {
            float c2 = -2.f * 0.7978845608028654f * 1.4426950408889634f * 0.044715f;
            asm volatile("" : "+v"(c2));
            G4 g;
            unsigned kvn = 0;
            SBAR();
            static_for<0, 112>([&](auto mc) {
                constexpr int m = decltype(mc)::value, s = (m % 56) >> 1, q = m - 60;
                if constexpr (m == 20) __builtin_amdgcn_s_setprio(1);
                if constexpr (m == 30) { wait_vm<0>(); asm volatile("s_mov_b64 exec, 1\n\tds_add_rtn_u32 %0, %1, %2\n\ts_mov_b64 exec, -1" : "=&v"(kvn) : "v"(L0 + L_CNT + C_KV), "v"(1u) : "memory"); }
                if constexpr (m == 38) {
                    asm volatile("" : "+v"(a0), "+v"(a1)); unsigned n_; asm volatile("s_waitcnt lgkmcnt(12)\n\tv_readfirstlane_b32 %0, %1" : "=s"(n_) : "v"(kvn) : "memory");
                    if (n_ == 7) set_flag(ws, WS_FKV + (head * 16 + r) * 128, tag, lane); }
                f32x16& acc = m < 56 ? ((m & 1) ? a1 : a0) : ((m & 1) ? c1 : c0);
                acc = MFMA(wf[m & 7], xf[s], s ? acc : zero16);
                SBAR();
                if constexpr (m + 6 < 112) rd((m + 6) ^ 1);
                if constexpr (q >= 0 && q < 36) { if constexpr (q < 18) geglu_step<q % 9, 4 * ((q / 9) & 1)>(g, a0, c2, h0); else geglu_step<q % 9, 4 * ((q / 9) & 1)>(g, a1, c2, h1); }
                if constexpr (m < 4) { v_pack(2 * m); v_pack(2 * m + 1); }
                if constexpr (m == 4) st128(ws, kv_off + 2048, l16, v0);
                if constexpr (m == 5) st128(ws, kv_off + 3072, l16, v1);
                if constexpr (q == 38) st128(ws, h_off + 2048, l16, h0);
                if constexpr (q == 42) st128(ws, h_off + 3072, l16, h1);
                if constexpr (q == 46) mark_o();
                SBAR();
            });
            asm volatile("" : "+v"(c0), "+v"(c1));
            arrive(C_B);
            h0 = geglu(c0); h1 = geglu(c1);
            asm volatile("" : "+v"(h0), "+v"(h1));
            __builtin_amdgcn_s_setprio(0);
            st128(ws, h_off + 4096, l16, h0); st128(ws, h_off + 5120, l16, h1);
        }
        await(C_B, 8);
#pragma unroll
        for (int i = 0; i < 24; ++i) {
            if (i == 8) { wait_vm<8>(); h_done(); }
            dma_wd(i >> 3, i & 7); __builtin_amdgcn_s_sleep(2);
        }
        const lptr db = lds + (4 * (g16 >> 1) + q4) * 128 + (((2 * (g16 & 1) + (p4 >> 1)) ^ (((q4 >> 1) & 1) << 2)) << 4) + 8 * (p4 & 1);
        const lptr dA[2] = {opq(db + rg0), opq(db + rg1)}, dB[2] = {opq((lptr)((unsigned)(uintptr_t)dA[0] ^ 64u)), opq((lptr)((unsigned)(uintptr_t)dA[1] ^ 64u))};
        poll(ws, lane < 14 ? WS_FH + ((ft >> 1) * 16 + lane) * 128 : WS_FKV + ((lane < 16 ? lane : 0) * 16 + (ft >> 1)) * 128, tag);
        const unsigned ob = WS_O + (2 * ft * 16 + 4 * kq) * 2048;
        const unsigned pr_off = ob + (lane & 7) * 1024 + ((lane >> 3) & 1) * 32768 + 1020;
        unsigned pr = EMPTY;
        float gA = __uint_as_float(__builtin_amdgcn_raw_buffer_load_b32(rs, r31 * 4, WS_G + 2 * ft * 128, 16)), gB = __uint_as_float(__builtin_amdgcn_raw_buffer_load_b32(rs, r31 * 4, WS_G + 2 * ft * 128 + 128, 16));
        {
            u32x4 hA[28], hB[28];
            const unsigned hb = WS_H + (2 * ft * 112 + 28 * kq) * 1024;
            auto ldh = [&](int n) { hA[n] = __builtin_amdgcn_raw_buffer_load_b128(rs, l16, hb + n * 1024, 16); hB[n] = __builtin_amdgcn_raw_buffer_load_b128(rs, l16, hb + (112 + n) * 1024, 16); };
#pragma unroll
            for (int n = 0; n < HLA; ++n) { ldh(n); SBAR(); __builtin_amdgcn_s_sleep(4); SBAR(); }
            bf16x8 wa[3], wb[3];
            auto rd = [&](int n, int half) {
                const int sl = (n >> 2) % 3, o_ = (sl == 2 ? 8192 : 0) + (n & 3) * 2048;
                if (half == 0) wa[n % 3] = rdtr2(dA[sl != 0] + o_, 1024); else wb[n % 3] = rdtr2(dB[sl != 0] + o_, 1024);
            };
            rd(0, 0); rd(0, 1); rd(1, 0); rd(1, 1);
            SBAR();
            static_for<0, 28>([&](auto sc) {
                constexpr int s = decltype(sc)::value;
                constexpr auto nh = [](int a, int b_) { int n = 0; for (int i = a; i <= b_; ++i) if (i + HLA < 28) n += 2; return n; };
                if constexpr (s == PAUSE) { asm volatile("" : "+v"(a0), "+v"(a1), "+v"(c0), "+v"(c1));
                    static_assert(PAUSE + HLA >= 28, "every h fragment is requested before the pause");
#pragma unroll
                    for (int n = PAUSE; n < 28; ++n) asm volatile("" : "+v"(hA[n]), "+v"(hB[n]));
                    asm volatile("" : "+v"(gA), "+v"(gB));
                    await(C_GO + 4 * t, 1, true); }
                if constexpr (s == 10 || s == 14 || s == 18) wait_vm<8 + nh(s - 5, s - 1)>();
                if constexpr (s == 22) wait_vm<8 + nh(17, 21)>();
                if constexpr (s == 2 || s == 6 || s == 10 || s == 14 || s == 18 || s == 22) asm volatile("s_waitcnt lgkmcnt(0)" ::: "memory");
                a0 = MFMA(wa[s % 3], __builtin_bit_cast(bf16x8, hA[s]), s ? a0 : zero16);
                SBAR();
                if constexpr (s < 26) rd(s + 2, 0);
                SBAR();
                a1 = MFMA(wb[s % 3], __builtin_bit_cast(bf16x8, hA[s]), s ? a1 : zero16);
                SBAR();
                if constexpr (s >= 2 && s < 18) { dma_wd(3 + (s - 2) / 4, 2 * ((s - 2) & 3)); dma_wd(3 + (s - 2) / 4, 2 * ((s - 2) & 3) + 1); }
                if constexpr (s >= 18 && s < 26) { dma_wo(2 * (s - 18)); if constexpr (s < 25) dma_wo(2 * (s - 18) + 1); }
                SBAR();
                c0 = MFMA(wa[s % 3], __builtin_bit_cast(bf16x8, hB[s]), s ? c0 : zero16);
                SBAR();
                if constexpr (s + HLA < 28) ldh(s + HLA);
                if constexpr (s == 26) asm volatile("global_load_dword %0, %1, %2 sc1" : "=v"(pr) : "v"(pr_off), "s"(ws) : "memory");
                SBAR();
                c1 = MFMA(wb[s % 3], __builtin_bit_cast(bf16x8, hB[s]), s ? c1 : zero16);
                SBAR();
                if constexpr (s < 26) rd(s + 2, 1);
                if constexpr (s < PAUSE) __builtin_amdgcn_s_sleep(2);
                SBAR();
            });
            asm volatile("" : "+v"(a0), "+v"(a1), "+v"(c0), "+v"(c1));
        }
        arrive(C_FIN);
        a0 *= 1.f - gA; a1 *= 1.f - gA; c0 *= 1.f - gB; c1 *= 1.f - gB;
        asm volatile("" : "+v"(a0), "+v"(a1), "+v"(c0), "+v"(c1));
        {
            wait_vm<0>();
            u64 late; asm volatile("v_cmp_eq_u32 %0, -1, %1\n\ts_nop 0" : "=s"(late) : "v"(pr) : "memory");
            if (late) {
#pragma nounroll
                for (unsigned spins = 0; spins < SPIN_CAP; ++spins) {
                    unsigned v; asm volatile("global_load_dword %0, %1, %2 sc1\n\ts_waitcnt vmcnt(0)" : "=&v"(v) : "v"(pr_off), "s"(ws) : "memory");
                    if (__all(v != EMPTY)) break;
                    __builtin_amdgcn_s_sleep(1);
                }
            }
            u32x4 of[16];
            auto ldo = [&](int i) { of[i] = __builtin_amdgcn_raw_buffer_load_b128(rs, l16 + (i & 3) * 1024, ob + ((i >> 2) & 1) * 4096 + (i >> 3) * 32768, 16); };
#pragma unroll
            for (int i = 0; i < 16; ++i) ldo(i);
            bf16x8 wa[8], wb[8];
#pragma unroll
            for (int e = 0; e < 8; ++e) { const int o_ = (28 * (e >> 1) + 16 * (e & 1)) * 128; wa[e] = rdtr2(dA[1] + o_, 1024); wb[e] = rdtr2(dB[1] + o_, 1024); }
            await(C_FIN, 8);
            const unsigned wb_ = L_RED + kq * 16384 + r31 * 256;
            auto put4 = [&](const f32x16& v, int i, unsigned o_, int c8) {
                *(LAS f32x4*)(smem + wb_ + o_ + (((c8 + 2 * i + hh) ^ (r31 & 7)) << 4)) = f32x4{v[4 * i], v[4 * i + 1], v[4 * i + 2], v[4 * i + 3]}; };
            SBAR();
            static_for<0, 4>([&](auto gc) {
                constexpr int g_ = decltype(gc)::value;
                auto empty = [&]() { unsigned m_ = 0;
#pragma unroll
                    for (int i = 4 * g_; i < 4 * g_ + 4; ++i) m_ = max(max(m_, max(of[i][0], of[i][1])), max(of[i][2], of[i][3]));
                    return __any(m_ == EMPTY); };
                if (__builtin_expect(empty(), 0)) {
#pragma nounroll
                    for (unsigned spins = 0; spins < SPIN_CAP; ++spins) {
#pragma unroll
                        for (int i = 4 * g_; i < 4 * g_ + 4; ++i)
                            asm volatile("global_load_dwordx4 %0, %1, %2 sc1\n\ts_waitcnt vmcnt(0)" : "=&v"(of[i]) : "v"(l16 + ob + (i & 7) * 1024 + (i >> 3) * 32768), "s"(ws) : "memory");
                        if (!empty()) break;
                    }
                }
#pragma unroll
                for (int i = 4 * g_; i < 4 * g_ + 4; ++i) {
                    if constexpr (g_ < 2) { a0 = MFMA(wa[i], __builtin_bit_cast(bf16x8, of[i]), a0); a1 = MFMA(wb[i], __builtin_bit_cast(bf16x8, of[i]), a1); }
                    else {
                        c0 = MFMA(wa[i - 8], __builtin_bit_cast(bf16x8, of[i]), c0); c1 = MFMA(wb[i - 8], __builtin_bit_cast(bf16x8, of[i]), c1);
                        if constexpr (g_ == 3) { SBAR(); put4(a0, i - 12, 0, 0); put4(a1, i - 12, 0, 8); SBAR(); }
                    }
                }
                asm volatile("" : "+v"(a0), "+v"(a1), "+v"(c0), "+v"(c1));
            });
#pragma unroll
            for (int i = 0; i < 4; ++i) { put4(c0, i, 8192, 0); put4(c1, i, 8192, 8); }
        }
        finish();
        }
    }
}

extern "C" void kernel_launch(void* const* d_in, const int* in_sizes, int n_in, void* d_out, int out_size, void* d_ws, size_t ws_size, hipStream_t stream) {
    (void)in_sizes; (void)n_in; (void)out_size; (void)ws_size;
    (void)hipFuncSetAttribute((const void*)fused_kernel, hipFuncAttributeMaxDynamicSharedMemorySize, LDS_BYTES);
    fused_kernel<<<256, 512, LDS_BYTES, stream>>>((const char*)d_in[0], (const char*)d_in[1], (const char*)d_in[2], (const char*)d_in[4], (const char*)d_in[5],
                                                  (const char*)d_in[6], (const char*)d_in[7], (char*)d_out, (char*)d_ws);
}
```
